```python
import torch

def _softmax(x, axis=-1):
    return torch.softmax(x, dim=axis)
def _gelu(x):
    return torch.nn.functional.gelu(x, approximate="tanh")
def _silu(x):
    return torch.nn.functional.silu(x)
def _sigmoid(x):
    return torch.sigmoid(x)
def _layernorm(x, axis=-1, eps=1e-5):
    m = x.mean(dim=axis, keepdim=True)
    v = x.var(dim=axis, keepdim=True, unbiased=False)
    return (x - m) / torch.sqrt(v + eps)
def _scatter_add(tgt, idx, src, axis=0):
    return tgt.clone().index_add_(axis, idx, src)
def _segment_sum(src, seg, n):
    out = torch.zeros((n,) + src.shape[1:], dtype=src.dtype)
    return out.index_add_(0, seg, src)
def _pad(x, pw):
    flat = tuple(v for pair in reversed(pw) for v in pair)
    return torch.nn.functional.pad(x, flat)

kernel_name = 'standalone__gqa_softmax__near_ridge'

def setup_inputs():

    _w = (lambda a: (lambda t: t.to(torch.bfloat16) if t.is_floating_point() else t)(torch.as_tensor(a)))
    B, Hq, Hkv, Sq, Sk, D = 8, 16, 8, 144, 1024, 256
    return {
        "q": _w((lambda *s: torch.randn(*s, dtype=torch.bfloat16) * 0.8)(B, Hq, Sq, D)),
        "k": _w((lambda *s: torch.randn(*s, dtype=torch.bfloat16) * 0.8)(B, Hkv, Sk, D)),
        "v": _w((lambda *s: torch.randn(*s, dtype=torch.bfloat16) * 0.05)(B, Hkv, Sk, D)),
    }

def reference(q, k, v):
    B, Hq, Sq, D = q.shape
    Hkv = k.shape[1]
    rep = Hq // Hkv
    k = (lambda x, n, axis: torch.repeat_interleave(x, n, dim=axis))(k, rep, axis=1)
    v = (lambda x, n, axis: torch.repeat_interleave(x, n, dim=axis))(v, rep, axis=1)
    scale = D ** -0.5
    scores = torch.einsum("bhqd,bhkd->bhqk", q, k) * scale
    p = _softmax(scores, axis=-1)
    return torch.einsum("bhqk,bhkd->bhqd", p, v)
```

```cpp
#include <hip/hip_runtime.h>
#include <hip/hip_bf16.h>

typedef __attribute__((ext_vector_type(8))) __bf16 bf16x8;
typedef __attribute__((ext_vector_type(2))) __bf16 bf16x2;
typedef __attribute__((ext_vector_type(4))) float f32x4;
typedef __attribute__((ext_vector_type(2))) float f32x2;
typedef __attribute__((ext_vector_type(4))) short s16x4;
typedef __attribute__((ext_vector_type(8))) short s16x8;
typedef __attribute__((ext_vector_type(4))) unsigned u32x4;
typedef __attribute__((ext_vector_type(2))) unsigned u32x2;
#define LDS_AS __attribute__((address_space(3)))
#define DI __device__ __forceinline__

constexpr int NB = 8, HQ = 16, HKV = 8, SQ = 144, SK = 1024, DH = 256;
constexpr int ROWS_KV = (HQ / HKV) * SQ;
constexpr int ROWS_WG = ROWS_KV / 4;
constexpr int NRT = 5;
constexpr int KT = 64;
constexpr int NT = SK / KT;
constexpr int ROWB = DH * 2;

constexpr int KSLOT = 2 * 4096 + 64;
constexpr int VSLOT = 8 * 1056;
constexpr int PBUF = NRT * 2 * 1024;
constexpr int OFF_K = 0;
constexpr int OFF_V0 = OFF_K + 8 * KSLOT;
constexpr int OFF_V1 = OFF_V0 + 4 * VSLOT;
constexpr int OFF_P = OFF_V1 + 4 * VSLOT;
constexpr int OFF_MX = OFF_P + 2 * PBUF;
constexpr int OFF_L = OFF_MX + 2 * 80 * 4;
constexpr int OFF_PF = OFF_L + 80 * 16 * 4;
constexpr int LDS_BYTES = OFF_PF + 256;
constexpr int OFF_Q = OFF_V1;
static_assert(OFF_Q + NRT * KSLOT <= OFF_MX, "Q staging too large");
static_assert(LDS_BYTES <= 163840, "LDS");

constexpr float SC_LOG2E = 0.0625f * 1.4426950408889634f;
constexpr float RESCALE_THR = 8.0f / SC_LOG2E;

#define PF_DIST 3

DI unsigned rfl(unsigned x) { return __builtin_amdgcn_readfirstlane(x); }
DI void dma16(unsigned m0v, unsigned voff, __amdgpu_buffer_rsrc_t srd, unsigned soff) {
  asm volatile("s_mov_b32 m0, %0\n\ts_nop 0\n\tbuffer_load_dwordx4 %1, %2, %3 offen lds"
               :: "s"(m0v), "v"(voff), "s"(srd), "s"(soff) : "memory");
}
DI void dma4(unsigned m0v, unsigned voff, __amdgpu_buffer_rsrc_t srd, unsigned soff) {
  asm volatile("s_mov_b32 m0, %0\n\ts_nop 0\n\tbuffer_load_dword %1, %2, %3 offen lds"
               :: "s"(m0v), "v"(voff), "s"(srd), "s"(soff) : "memory");
}
template <int N> DI void wait_vm() { asm volatile("s_waitcnt vmcnt(%0)" :: "i"(N) : "memory"); }
DI void wg_barrier() {
  asm volatile("s_waitcnt lgkmcnt(0)\n\ts_barrier" ::: "memory");
}
#define SB0() __builtin_amdgcn_sched_barrier(0)
DI float fmax2(float a, float b) { return __builtin_elementwise_maximum(a, b); }
DI unsigned pk2(float a, float b) {
  f32x2 v = {a, b};
  return __builtin_bit_cast(unsigned, __builtin_convertvector(v, bf16x2));
}
DI f32x4 mfma16(bf16x8 a, bf16x8 b, f32x4 c) { return __builtin_amdgcn_mfma_f32_16x16x32_bf16(a, b, c, 0, 0, 0); }

__global__ __launch_bounds__(512, 2) void gqa_fwd(const __bf16* __restrict__ Q, const __bf16* __restrict__ K,
                                                  const __bf16* __restrict__ V, __bf16* __restrict__ O) {
  __shared__ __attribute__((aligned(16))) char smem[LDS_BYTES];
  LDS_AS char* lds = (LDS_AS char*)smem;
  const unsigned lds0 = (unsigned)(size_t)lds;
  const int tid = threadIdx.x;
  const int lane = tid & 63;
  const int wave = rfl(tid >> 6);
  const int w = wave & 3;
  const int r16 = lane & 15, g = lane >> 4;
  const int bid = blockIdx.x;
  const int bh = (bid & 7) * 8 + (bid >> 5);
  const int jq = (bid >> 3) & 3;
  const int row0 = bh * ROWS_KV + jq * ROWS_WG;

  const __amdgpu_buffer_rsrc_t srdQ = __builtin_amdgcn_make_buffer_rsrc((void*)(Q + (size_t)row0 * DH), (short)0, ROWS_WG * ROWB, 0x00020000);
  const __amdgpu_buffer_rsrc_t srdK = __builtin_amdgcn_make_buffer_rsrc((void*)(K + (size_t)bh * SK * DH), (short)0, SK * ROWB, 0x00020000);
  const __amdgpu_buffer_rsrc_t srdV = __builtin_amdgcn_make_buffer_rsrc((void*)(V + (size_t)bh * SK * DH), (short)0, SK * ROWB, 0x00020000);

  const unsigned vsrc_row = (lane >> 3) * ROWB + (((lane & 7) ^ ((lane >> 4) & 3)) << 4);
  if (tid < 160) *(LDS_AS float*)(lds + OFF_MX + tid * 4) = -INFINITY;

  asm volatile("s_nop 4" ::: "memory");
#pragma unroll
  for (int i = 0; i < 5; ++i) {
    const int pc = wave * 5 + i;
    const int rb = pc >> 2, cb = pc & 3;
    const int rbs = rb > 8 ? 8 : rb;
    dma16(rfl(lds0 + OFF_Q + (rb >> 1) * KSLOT + (rb & 1) * 4160 + cb * 1024), vsrc_row, srdQ, rfl(rbs * 8 * ROWB + cb * 128));
  }

  if (wave < 4) {
    const unsigned kbase = lds0 + OFF_K + w * 2 * KSLOT;
    auto dma_k = [&](int t, int pc) {
      const int rb = pc & 1, cb = pc >> 1;
      dma16(kbase + (t & 1) * KSLOT + rb * 4160 + cb * 1024, vsrc_row, srdK, (t * KT + w * 16 + rb * 8) * ROWB + cb * 128);
    };
#pragma unroll
    for (int pc = 0; pc < 8; ++pc) dma_k(0, pc);
#pragma unroll
    for (int pc = 0; pc < 8; ++pc) dma_k(1, pc);
    wait_vm<16>();
    wg_barrier();
    const int frag_off = (r16 >> 3) * 4160 + (r16 & 7) * 128 + ((g ^ ((r16 >> 1) & 3)) << 4);
    bf16x8 qf[NRT][8];
#pragma unroll
    for (int rt = 0; rt < NRT; ++rt)
#pragma unroll
      for (int s = 0; s < 8; ++s)
        qf[rt][s] = *(LDS_AS bf16x8*)(lds + OFF_Q + rt * KSLOT + frag_off + (s >> 1) * 1024 + (s & 1) * 64);
    LDS_AS char* kp = lds + OFF_K + w * 2 * KSLOT + frag_off;
    LDS_AS char* pp = lds + OFF_P + (w >> 1) * 1024 + lane * 16 + (w & 1) * 8;
    LDS_AS float* mxp = (LDS_AS float*)(lds + OFF_MX) + r16;
    float mref[NRT], lsum[NRT];
    f32x4 sacc[NRT];
#pragma unroll
    for (int rt = 0; rt < NRT; ++rt) { mref[rt] = -INFINITY; lsum[rt] = 0.f; }

    auto softmax = [&](int par) {
      float mx[NRT];
#pragma unroll
      for (int rt = 0; rt < NRT; ++rt) mx[rt] = mxp[par * 80 + rt * 16];
      bool grow = false;
#pragma unroll
      for (int rt = 0; rt < NRT; ++rt) grow |= mx[rt] > mref[rt] + RESCALE_THR;
      if (__builtin_amdgcn_ballot_w64(grow)) {
#pragma unroll
        for (int rt = 0; rt < NRT; ++rt) {
          const float mn = (mx[rt] > mref[rt] + RESCALE_THR) ? mx[rt] : mref[rt];
          lsum[rt] *= __builtin_amdgcn_exp2f((mref[rt] - mn) * SC_LOG2E);
          mref[rt] = mn;
        }
      }
#pragma unroll
      for (int rt = 0; rt < NRT; ++rt) {
        const float mc = -mref[rt] * SC_LOG2E;
        float p[4];
#pragma unroll
        for (int i = 0; i < 4; ++i) p[i] = __builtin_amdgcn_exp2f(__builtin_fmaf(sacc[rt][i], SC_LOG2E, mc));
        lsum[rt] += (p[0] + p[1]) + (p[2] + p[3]);
        u32x2 pk = {pk2(p[0], p[1]), pk2(p[2], p[3])};
        *(LDS_AS u32x2*)(pp + par * PBUF + rt * 2048) = pk;
      }
    };
    auto qk = [&](int t, int par, bool more) {
#pragma unroll
      for (int rt = 0; rt < NRT; ++rt) sacc[rt] = f32x4{0.f, 0.f, 0.f, 0.f};
      bf16x8 kf[8];
      kf[0] = *(LDS_AS bf16x8*)(kp + par * KSLOT);
      kf[1] = *(LDS_AS bf16x8*)(kp + par * KSLOT + 64);
#pragma unroll
      for (int s = 0; s < 8; ++s) {
        if (s + 2 < 8) kf[s + 2] = *(LDS_AS bf16x8*)(kp + par * KSLOT + ((s + 2) >> 1) * 1024 + (s & 1) * 64);
#pragma unroll
        for (int rt = 0; rt < NRT; ++rt) sacc[rt] = mfma16(kf[s], qf[rt][s], sacc[rt]);
        SB0();
        if (more && s >= 1) dma_k(t + 2, s - 1);
        SB0();
      }
      if (more) dma_k(t + 2, 7);
#pragma unroll
      for (int rt = 0; rt < NRT; ++rt) {
        const float m = fmax2(fmax2(sacc[rt][0], sacc[rt][1]), fmax2(sacc[rt][2], sacc[rt][3]));
        __hip_atomic_fetch_max(mxp + par * 80 + rt * 16, m, __ATOMIC_RELAXED, __HIP_MEMORY_SCOPE_WORKGROUP);
      }
    };

    wait_vm<8>();
    qk(0, 0, true);
    wg_barrier();
#pragma unroll 1
    for (int t = 1; t < NT - 3; t += 2) {
      softmax(0); SB0();
      wait_vm<8>();
      qk(t, 1, true);
      wg_barrier();
      softmax(1); SB0();
      wait_vm<8>();
      qk(t + 1, 0, true);
      wg_barrier();
    }
    softmax(0); SB0(); wait_vm<8>(); qk(13, 1, true);  wg_barrier();
    softmax(1); SB0(); wait_vm<8>(); qk(14, 0, false); wg_barrier();
    softmax(0); SB0(); wait_vm<0>(); qk(15, 1, false); wg_barrier();
    softmax(1);
#pragma unroll
    for (int rt = 0; rt < NRT; ++rt) *(LDS_AS float*)(lds + OFF_L + ((rt * 16 + r16) * 16 + w * 4 + g) * 4) = lsum[rt];
    wg_barrier();
  } else {
    const unsigned vsrc_v = (16 * (lane >> 4) + ((lane >> 3) & 1)) * ROWB + (lane & 7) * 16 + w * 128;
    const unsigned vbase = lds0 + OFF_V0 + w * VSLOT;
    auto dma_v = [&](int t, int pc) {
      dma16(vbase + (t & 1) * (4 * VSLOT) + pc * 1056, vsrc_v, srdV, t * KT * ROWB + pc * 1024);
    };
    const unsigned vsrc_pf = (jq * 64 + lane) * 128 + (w & 1) * 64;
    const __amdgpu_buffer_rsrc_t srdPF = __builtin_amdgcn_make_buffer_rsrc((void*)((w < 2 ? K : V) + (size_t)bh * SK * DH), (short)0, SK * ROWB, 0x00020000);
    const int pf_add = w < 2 ? 2 : 0;
    auto prefetch = [&](int n) {
      const int tt = n + pf_add < NT ? n + pf_add : NT - 1;
      dma4(lds0 + OFF_PF, vsrc_pf, srdPF, tt * KT * ROWB);
    };
#pragma unroll
    for (int pc = 0; pc < 8; ++pc) dma_v(0, pc);
    wait_vm<8>();
    wg_barrier();
    const int kk = 4 * g + (r16 >> 2), pq = lane & 3;
    LDS_AS char* vp = lds + OFF_V0 + w * VSLOT + (kk >> 1) * 1056 + (kk & 1) * 128 + pq * 8;
    LDS_AS char* pp = lds + OFF_P + lane * 16;
    LDS_AS float* mxp = (LDS_AS float*)(lds + OFF_MX) + r16;
    f32x4 oacc[4][NRT];
    float mref[NRT];
#pragma unroll
    for (int rt = 0; rt < NRT; ++rt) {
      mref[rt] = -INFINITY;
#pragma unroll
      for (int c = 0; c < 4; ++c) oacc[c][rt] = f32x4{0.f, 0.f, 0.f, 0.f};
    }
    bf16x8 vf[2][4];
    auto load_v = [&](int par) {
#pragma unroll
      for (int ks = 0; ks < 2; ++ks)
#pragma unroll
        for (int c = 0; c < 4; ++c) {
          const s16x4 a = __builtin_amdgcn_ds_read_tr16_b64_v4i16((LDS_AS s16x4*)(vp + par * (4 * VSLOT) + (2 * ks) * 256 + c * 32));
          const s16x4 b = __builtin_amdgcn_ds_read_tr16_b64_v4i16((LDS_AS s16x4*)(vp + par * (4 * VSLOT) + (2 * ks + 1) * 256 + c * 32));
          vf[ks][c] = __builtin_bit_cast(bf16x8, __builtin_shufflevector(a, b, 0, 1, 2, 3, 4, 5, 6, 7));
        }
    };
    auto rescale = [&](int par) {
      float mx[NRT];
#pragma unroll
      for (int rt = 0; rt < NRT; ++rt) mx[rt] = mxp[par * 80 + rt * 16];
      bool grow = false;
#pragma unroll
      for (int rt = 0; rt < NRT; ++rt) grow |= mx[rt] > mref[rt] + RESCALE_THR;
      if (__builtin_amdgcn_ballot_w64(grow)) {
#pragma unroll
        for (int rt = 0; rt < NRT; ++rt) {
          const float mn = (mx[rt] > mref[rt] + RESCALE_THR) ? mx[rt] : mref[rt];
          const float al = __builtin_amdgcn_exp2f((mref[rt] - mn) * SC_LOG2E);
          mref[rt] = mn;
#pragma unroll
          for (int c = 0; c < 4; ++c) oacc[c][rt] *= al;
        }
      }
    };
    auto pv = [&](int tn, int par, bool more) {
      bf16x8 pf[2][NRT];
#pragma unroll
      for (int ks = 0; ks < 2; ++ks)
#pragma unroll
        for (int rt = 0; rt < NRT; ++rt) pf[ks][rt] = *(LDS_AS bf16x8*)(pp + par * PBUF + rt * 2048 + ks * 1024);
      SB0();
#pragma unroll
      for (int ks = 0; ks < 2; ++ks)
#pragma unroll
        for (int rt = 0; rt < NRT; ++rt) {
#pragma unroll
          for (int c = 0; c < 4; ++c) oacc[c][rt] = mfma16(vf[ks][c], pf[ks][rt], oacc[c][rt]);
          SB0();
          const int i = ks * NRT + rt;
          if (more && i < 8) dma_v(tn, i);
          SB0();
        }
    };
#pragma unroll
    for (int n = 0; n <= PF_DIST; ++n) prefetch(n);
    constexpr int PFN = 1, PF0 = PF_DIST + 1;
    wg_barrier();
#pragma unroll
    for (int pc = 0; pc < 8; ++pc) dma_v(1, pc);
    rescale(0);
    wait_vm<8 + PF0>();
    load_v(0);
    prefetch(1 + PF_DIST);
    wg_barrier();
#pragma unroll 1
    for (int t = 2; t < NT; t += 2) {
      pv(t, 0, true);
      rescale(1);
      wait_vm<8 + PFN>();
      load_v(1);
      prefetch(t + PF_DIST);
      wg_barrier();
      pv(t + 1, 1, true);
      rescale(0);
      wait_vm<8 + PFN>();
      load_v(0);
      prefetch(t + 1 + PF_DIST);
      wg_barrier();
    }
    pv(0, 0, false);
    rescale(1);
    wait_vm<0>();
    load_v(1);
    wg_barrier();
    pv(0, 1, false);
#pragma unroll
    for (int rt = 0; rt < NRT; ++rt) {
      const LDS_AS f32x4* lp = (const LDS_AS f32x4*)(lds + OFF_L + (rt * 16 + r16) * 64);
      const f32x4 s4 = (lp[0] + lp[1]) + (lp[2] + lp[3]);
      const float inv = 1.0f / ((s4[0] + s4[1]) + (s4[2] + s4[3]));
      const int row = rt * 16 + r16;
#pragma unroll
      for (int u = 0; u < 2; ++u) {
        const f32x4 a = oacc[2 * u][rt] * inv, b = oacc[2 * u + 1][rt] * inv;
        const auto x = __builtin_amdgcn_permlane16_swap(pk2(a[0], a[1]), pk2(b[0], b[1]), false, false);
        const auto y = __builtin_amdgcn_permlane16_swap(pk2(a[2], a[3]), pk2(b[2], b[3]), false, false);
        const u32x4 o = {x[0], y[0], x[1], y[1]};
        if (row < ROWS_WG)
          *(u32x4*)(O + (size_t)(row0 + row) * DH + w * 64 + u * 32 + (g & 1) * 16 + (g >> 1) * 8) = o;
      }
    }
  }
}

extern "C" void kernel_launch(void* const* d_in, const int* in_sizes, int n_in, void* d_out, int out_size,
                              void* d_ws, size_t ws_size, hipStream_t stream) {
  (void)in_sizes; (void)n_in; (void)out_size; (void)d_ws; (void)ws_size;
  gqa_fwd<<<NB * HKV * 4, 512, 0, stream>>>((const __bf16*)d_in[0], (const __bf16*)d_in[1], (const __bf16*)d_in[2], (__bf16*)d_out);
}
```

```cpp
#include <hip/hip_runtime.h>
#include <hip/hip_bf16.h>

typedef __attribute__((ext_vector_type(8))) __bf16 bf16x8;
typedef __attribute__((ext_vector_type(2))) __bf16 bf16x2;
typedef __attribute__((ext_vector_type(4))) float f32x4;
typedef __attribute__((ext_vector_type(2))) float f32x2;
typedef __attribute__((ext_vector_type(4))) short s16x4;
typedef __attribute__((ext_vector_type(8))) short s16x8;
typedef __attribute__((ext_vector_type(4))) unsigned u32x4;
typedef __attribute__((ext_vector_type(2))) unsigned u32x2;
#define LDS_AS __attribute__((address_space(3)))
#define DI __device__ __forceinline__

constexpr int NB = 8, HQ = 16, HKV = 8, SQ = 144, SK = 1024, DH = 256;
constexpr int ROWS_KV = (HQ / HKV) * SQ;
constexpr int ROWS_WG = ROWS_KV / 4;
constexpr int NRT = 5;
constexpr int KT = 64;
constexpr int NT = SK / KT;
constexpr int ROWB = DH * 2;

constexpr int KSLOT = 2 * 4096 + 64;
constexpr int VSLOT = 8 * 1056;
constexpr int PBUF = NRT * 2 * 1024;
constexpr int OFF_K = 0;
constexpr int OFF_V0 = OFF_K + 8 * KSLOT;
constexpr int OFF_V1 = OFF_V0 + 4 * VSLOT;
constexpr int OFF_P = OFF_V1 + 4 * VSLOT;
constexpr int OFF_MX = OFF_P + 2 * PBUF;
constexpr int OFF_L = OFF_MX + 2 * 80 * 4;
constexpr int OFF_PF = OFF_L + 80 * 16 * 4;
constexpr int LDS_BYTES = OFF_PF + 256;
#define STAMP() do {} while (0)
#define STAMP_DUMP() do {} while (0)
constexpr int OFF_Q = OFF_V1;
static_assert(OFF_Q + NRT * KSLOT <= OFF_MX, "Q staging too large");
static_assert(LDS_BYTES <= 163840, "LDS");

constexpr float SC_LOG2E = 0.0625f * 1.4426950408889634f;
constexpr float RESCALE_THR = 8.0f / SC_LOG2E;

#define PF_DIST 0
#define POL "nt"
#ifndef NVA
#define NVA 3
#endif
#define PRIO_QK 0
#define PRIO_PV 0

DI unsigned rfl(unsigned x) { return __builtin_amdgcn_readfirstlane(x); }
DI void dma16(unsigned m0v, unsigned voff, __amdgpu_buffer_rsrc_t srd, unsigned soff) {
  asm volatile("s_mov_b32 m0, %0\n\ts_nop 0\n\tbuffer_load_dwordx4 %1, %2, %3 offen " POL " lds"
               :: "s"(m0v), "v"(voff), "s"(srd), "s"(soff) : "memory");
}
DI void dma4(unsigned m0v, unsigned voff, __amdgpu_buffer_rsrc_t srd, unsigned soff) {
  asm volatile("s_mov_b32 m0, %0\n\ts_nop 0\n\tbuffer_load_dword %1, %2, %3 offen lds"
               :: "s"(m0v), "v"(voff), "s"(srd), "s"(soff) : "memory");
}
template <int N> DI void wait_vm() { asm volatile("s_waitcnt vmcnt(%0)" :: "i"(N) : "memory"); }
DI void wg_barrier() {
  asm volatile("s_waitcnt lgkmcnt(0)\n\ts_barrier" ::: "memory");
}
#define SB0() __builtin_amdgcn_sched_barrier(0)
DI float fmax2(float a, float b) { return __builtin_elementwise_maximum(a, b); }
DI unsigned pk2(float a, float b) {
  f32x2 v = {a, b};
  return __builtin_bit_cast(unsigned, __builtin_convertvector(v, bf16x2));
}
DI f32x4 mfma16(bf16x8 a, bf16x8 b, f32x4 c) { return __builtin_amdgcn_mfma_f32_16x16x32_bf16(a, b, c, 0, 0, 0); }

__global__ __launch_bounds__(512, 2) void gqa_fwd(const __bf16* __restrict__ Q, const __bf16* __restrict__ K,
                                                  const __bf16* __restrict__ V, __bf16* __restrict__ O) {
  __shared__ __attribute__((aligned(16))) char smem[LDS_BYTES];
  LDS_AS char* lds = (LDS_AS char*)smem;
  const unsigned lds0 = (unsigned)(size_t)lds;
  const int tid = threadIdx.x;
  const int lane = tid & 63;
  const int wave = rfl(tid >> 6);
  const int w = wave & 3;
  const int r16 = lane & 15, g = lane >> 4;
  int stn = 0; (void)stn;
  STAMP();
  const int bid = blockIdx.x;
  const int bh = (bid & 7) * 8 + (bid >> 5);
  const int jq = (bid >> 3) & 3;
  const int row0 = bh * ROWS_KV + jq * ROWS_WG;

  const __amdgpu_buffer_rsrc_t srdQ = __builtin_amdgcn_make_buffer_rsrc((void*)(Q + (size_t)row0 * DH), (short)0, ROWS_WG * ROWB, 0x00020000);
  const __amdgpu_buffer_rsrc_t srdK = __builtin_amdgcn_make_buffer_rsrc((void*)(K + (size_t)bh * SK * DH), (short)0, SK * ROWB, 0x00020000);
  const __amdgpu_buffer_rsrc_t srdV = __builtin_amdgcn_make_buffer_rsrc((void*)(V + (size_t)bh * SK * DH), (short)0, SK * ROWB, 0x00020000);

  const unsigned vsrc_row = (lane >> 3) * ROWB + (((lane & 7) ^ ((lane >> 4) & 3)) << 4);
  if (tid < 160) *(LDS_AS float*)(lds + OFF_MX + tid * 4) = -INFINITY;

  asm volatile("s_nop 4" ::: "memory");
#pragma unroll
  for (int i = 0; i < 5; ++i) {
    const int pc = wave * 5 + i;
    const int rb = pc >> 2, cb = pc & 3;
    const int rbs = rb > 8 ? 8 : rb;
    dma16(rfl(lds0 + OFF_Q + (rb >> 1) * KSLOT + (rb & 1) * 4160 + cb * 1024), vsrc_row, srdQ, rfl(rbs * 8 * ROWB + cb * 128));
  }

  if (wave < 4) {
    const unsigned kbase = lds0 + OFF_K + w * 2 * KSLOT;
    auto dma_k = [&](int t, int pc) {
      const int rb = pc & 1, cb = pc >> 1;
      dma16(kbase + (t & 1) * KSLOT + rb * 4160 + cb * 1024, vsrc_row, srdK, (t * KT + w * 16 + rb * 8) * ROWB + cb * 128);
    };
#pragma unroll
    for (int pc = 0; pc < 8; ++pc) dma_k(0, pc);
#pragma unroll
    for (int pc = 0; pc < 8; ++pc) dma_k(1, pc);
    wait_vm<16>();
    wg_barrier();
    STAMP();
    const int frag_off = (r16 >> 3) * 4160 + (r16 & 7) * 128 + ((g ^ ((r16 >> 1) & 3)) << 4);
    bf16x8 qf[NRT][8];
#pragma unroll
    for (int rt = 0; rt < NRT; ++rt)
#pragma unroll
      for (int s = 0; s < 8; ++s)
        qf[rt][s] = *(LDS_AS bf16x8*)(lds + OFF_Q + rt * KSLOT + frag_off + (s >> 1) * 1024 + (s & 1) * 64);
    LDS_AS char* kp = lds + OFF_K + w * 2 * KSLOT + frag_off;
    LDS_AS char* pp = lds + OFF_P + (w >> 1) * 1024 + lane * 16 + (w & 1) * 8;
    LDS_AS float* mxp = (LDS_AS float*)(lds + OFF_MX) + r16;
    float mthr[NRT], mcs[NRT], lsum[NRT], mx[NRT];
    f32x4 sacc[2][NRT];
    bf16x8 kf0, kf1;
#pragma unroll
    for (int rt = 0; rt < NRT; ++rt) { mthr[rt] = -INFINITY; mcs[rt] = INFINITY; lsum[rt] = 0.f; }
    if (PRIO_QK) __builtin_amdgcn_s_setprio(PRIO_QK);

    auto mxread = [&](int par) {
#pragma unroll
      for (int rt = 0; rt < NRT; ++rt) mx[rt] = mxp[par * 80 + rt * 16];
    };
    auto check = [&]() {
      bool grow = false;
#pragma unroll
      for (int rt = 0; rt < NRT; ++rt) grow |= mx[rt] > mthr[rt];
      if (__builtin_amdgcn_ballot_w64(grow)) {
#pragma unroll
        for (int rt = 0; rt < NRT; ++rt) {
          const bool gr = mx[rt] > mthr[rt];
          const float mcn = gr ? -mx[rt] * SC_LOG2E : mcs[rt];
          lsum[rt] *= __builtin_amdgcn_exp2f(mcn - mcs[rt]);
          mcs[rt] = mcn;
          mthr[rt] = gr ? mx[rt] + RESCALE_THR : mthr[rt];
        }
      }
    };
    auto sm_rt = [&](int par, int rt) {
      const float mc = mcs[rt];
      float p[4];
#pragma unroll
      for (int i = 0; i < 4; ++i) p[i] = __builtin_amdgcn_exp2f(__builtin_fmaf(sacc[par][rt][i], SC_LOG2E, mc));
      float a = p[0] + p[1], b = p[2] + p[3];
      asm volatile("" : "+v"(a));
      lsum[rt] += a + b;
      u32x2 pk = {pk2(p[0], p[1]), pk2(p[2], p[3])};
      *(LDS_AS u32x2*)(pp + par * PBUF + rt * 2048) = pk;
    };
    auto kread = [&](int par, int s) { return *(LDS_AS bf16x8*)(kp + par * KSLOT + (s >> 1) * 1024 + (s & 1) * 64); };
    auto iter = [&](int t, int par, bool sm, bool more, int nextwait) {
      bf16x8 kf[8];
      kf[0] = kf0; kf[1] = kf1;
      if (sm) mxread(par ^ 1);
#pragma unroll
      for (int s = 0; s < 8; ++s) {
        if (s + 2 < 8) kf[s + 2] = kread(par, s + 2);
#pragma unroll
        for (int rt = 0; rt < NRT; ++rt) sacc[par][rt] = mfma16(kf[s], qf[rt][s], s == 0 ? f32x4{0.f, 0.f, 0.f, 0.f} : sacc[par][rt]);
        if (sm && s == 1) { SB0(); check(); }
        if (sm && s >= 2 && s <= 6) {
          sm_rt(par ^ 1, s - 2);
#pragma unroll
          for (int i = 0; i < 5; ++i) {
            __builtin_amdgcn_sched_group_barrier(0x008, 1, 0);
            __builtin_amdgcn_sched_group_barrier(0x002, NVA, 0);
          }
        }
        SB0();
        if (more && s >= 1) dma_k(t + 2, s - 1);
        SB0();
      }
      if (more) dma_k(t + 2, 7);
      if (nextwait == 8) wait_vm<8>(); else if (nextwait == 0) wait_vm<0>();
      if (nextwait >= 0) { kf0 = kread(par ^ 1, 0); kf1 = kread(par ^ 1, 1); }
#pragma unroll
      for (int rt = 0; rt < NRT; ++rt) {
        const float m = fmax2(fmax2(sacc[par][rt][0], sacc[par][rt][1]), fmax2(sacc[par][rt][2], sacc[par][rt][3]));
        __hip_atomic_fetch_max(mxp + par * 80 + rt * 16, m, __ATOMIC_RELAXED, __HIP_MEMORY_SCOPE_WORKGROUP);
      }
    };
    STAMP();
    wait_vm<8>();
    kf0 = kread(0, 0); kf1 = kread(0, 1);
    STAMP();
    iter(0, 0, false, true, 8);
    STAMP();
    wg_barrier();
#pragma unroll 1
    for (int t = 1; t < NT - 3; t += 2) {
      STAMP();
      iter(t, 1, true, true, 8);
      STAMP();
      wg_barrier();
      STAMP();
      iter(t + 1, 0, true, true, 8);
      STAMP();
      wg_barrier();
    }
    iter(13, 1, true, true, 8);   wg_barrier();
    iter(14, 0, true, false, 0);  wg_barrier();
    iter(15, 1, true, false, -1); wg_barrier();
    mxread(1);
    check();
#pragma unroll
    for (int rt = 0; rt < NRT; ++rt) sm_rt(1, rt);
#pragma unroll
    for (int rt = 0; rt < NRT; ++rt) *(LDS_AS float*)(lds + OFF_L + ((rt * 16 + r16) * 16 + w * 4 + g) * 4) = lsum[rt];
    wg_barrier();
    STAMP();
    STAMP_DUMP();
  } else {
    const unsigned vsrc_v = (16 * (lane >> 4) + ((lane >> 3) & 1)) * ROWB + (lane & 7) * 16 + w * 128;
    const unsigned vbase = lds0 + OFF_V0 + w * VSLOT;
    auto dma_v = [&](int t, int pc) {
      dma16(vbase + (t & 1) * (4 * VSLOT) + pc * 1056, vsrc_v, srdV, t * KT * ROWB + pc * 1024);
    };
    const unsigned vsrc_pf = (jq * 64 + lane) * 128 + (w & 1) * 64;
    const __amdgpu_buffer_rsrc_t srdPF = __builtin_amdgcn_make_buffer_rsrc((void*)((w < 2 ? K : V) + (size_t)bh * SK * DH), (short)0, SK * ROWB, 0x00020000);
    const int pf_add = w < 2 ? 2 : 0;
    auto prefetch = [&](int n) {
    };
#pragma unroll
    for (int pc = 0; pc < 8; ++pc) dma_v(0, pc);
    wait_vm<8>();
    wg_barrier();
    STAMP();
    const int kk = 4 * g + (r16 >> 2), pq = lane & 3;
    LDS_AS char* vp = lds + OFF_V0 + w * VSLOT + (kk >> 1) * 1056 + (kk & 1) * 128 + pq * 8;
    LDS_AS char* pp = lds + OFF_P + lane * 16;
    LDS_AS float* mxp = (LDS_AS float*)(lds + OFF_MX) + r16;
    if (PRIO_PV) __builtin_amdgcn_s_setprio(PRIO_PV);
    f32x4 oacc[4][NRT];
    float mthr[NRT], mcs[NRT];
#pragma unroll
    for (int rt = 0; rt < NRT; ++rt) {
      mthr[rt] = -INFINITY; mcs[rt] = INFINITY;
#pragma unroll
      for (int c = 0; c < 4; ++c) oacc[c][rt] = f32x4{0.f, 0.f, 0.f, 0.f};
    }
    bf16x8 vf[2][4];
    auto load_v = [&](int par) {
#pragma unroll
      for (int ks = 0; ks < 2; ++ks)
#pragma unroll
        for (int c = 0; c < 4; ++c) {
          const s16x4 a = __builtin_amdgcn_ds_read_tr16_b64_v4i16((LDS_AS s16x4*)(vp + par * (4 * VSLOT) + (2 * ks) * 256 + c * 32));
          const s16x4 b = __builtin_amdgcn_ds_read_tr16_b64_v4i16((LDS_AS s16x4*)(vp + par * (4 * VSLOT) + (2 * ks + 1) * 256 + c * 32));
          vf[ks][c] = __builtin_bit_cast(bf16x8, __builtin_shufflevector(a, b, 0, 1, 2, 3, 4, 5, 6, 7));
        }
    };
    auto rescale = [&](int par) {
      float mx[NRT];
#pragma unroll
      for (int rt = 0; rt < NRT; ++rt) mx[rt] = mxp[par * 80 + rt * 16];
      bool grow = false;
#pragma unroll
      for (int rt = 0; rt < NRT; ++rt) grow |= mx[rt] > mthr[rt];
      if (__builtin_amdgcn_ballot_w64(grow)) {
#pragma unroll
        for (int rt = 0; rt < NRT; ++rt) {
          const bool gr = mx[rt] > mthr[rt];
          const float mcn = gr ? -mx[rt] * SC_LOG2E : mcs[rt];
          const float al = __builtin_amdgcn_exp2f(mcn - mcs[rt]);
          mcs[rt] = mcn;
          mthr[rt] = gr ? mx[rt] + RESCALE_THR : mthr[rt];
#pragma unroll
          for (int c = 0; c < 4; ++c) oacc[c][rt] *= al;
        }
      }
    };
    auto pv = [&](int tn, int par, bool more) {
      bf16x8 pf[2][NRT];
#pragma unroll
      for (int ks = 0; ks < 2; ++ks)
#pragma unroll
        for (int rt = 0; rt < NRT; ++rt) pf[ks][rt] = *(LDS_AS bf16x8*)(pp + par * PBUF + rt * 2048 + ks * 1024);
      SB0();
#pragma unroll
      for (int ks = 0; ks < 2; ++ks)
#pragma unroll
        for (int rt = 0; rt < NRT; ++rt) {
#pragma unroll
          for (int c = 0; c < 4; ++c) oacc[c][rt] = mfma16(vf[ks][c], pf[ks][rt], oacc[c][rt]);
          SB0();
          const int i = ks * NRT + rt;
          if (more && i < 8) dma_v(tn, i);
          SB0();
        }
    };
    constexpr int PFN = 0, PF0 = 0;
    wg_barrier();
#pragma unroll
    for (int pc = 0; pc < 8; ++pc) dma_v(1, pc);
    rescale(0);
    wait_vm<8 + PF0>();
    load_v(0);
    prefetch(1 + PF_DIST);
    wg_barrier();
#pragma unroll 1
    for (int t = 2; t < NT; t += 2) {
      STAMP();
      pv(t, 0, true);
      rescale(1);
      wait_vm<8 + PFN>();
      load_v(1);
      prefetch(t + PF_DIST);
      STAMP();
      wg_barrier();
      STAMP();
      pv(t + 1, 1, true);
      rescale(0);
      wait_vm<8 + PFN>();
      load_v(0);
      prefetch(t + 1 + PF_DIST);
      STAMP();
      wg_barrier();
    }
    pv(0, 0, false);
    rescale(1);
    wait_vm<0>();
    load_v(1);
    wg_barrier();
    pv(0, 1, false);
    STAMP();
#pragma unroll
    for (int rt = 0; rt < NRT; ++rt) {
      const LDS_AS f32x4* lp = (const LDS_AS f32x4*)(lds + OFF_L + (rt * 16 + r16) * 64);
      const f32x4 s4 = (lp[0] + lp[1]) + (lp[2] + lp[3]);
      const float inv = 1.0f / ((s4[0] + s4[1]) + (s4[2] + s4[3]));
      const int row = rt * 16 + r16;
#pragma unroll
      for (int u = 0; u < 2; ++u) {
        const f32x4 a = oacc[2 * u][rt] * inv, b = oacc[2 * u + 1][rt] * inv;
        const auto x = __builtin_amdgcn_permlane16_swap(pk2(a[0], a[1]), pk2(b[0], b[1]), false, false);
        const auto y = __builtin_amdgcn_permlane16_swap(pk2(a[2], a[3]), pk2(b[2], b[3]), false, false);
        const u32x4 o = {x[0], y[0], x[1], y[1]};
        if (row < ROWS_WG)
          *(u32x4*)(O + (size_t)(row0 + row) * DH + w * 64 + u * 32 + (g & 1) * 16 + (g >> 1) * 8) = o;
      }
    }
    STAMP();
    STAMP_DUMP();
  }
}

extern "C" void kernel_launch(void* const* d_in, const int* in_sizes, int n_in, void* d_out, int out_size,
                              void* d_ws, size_t ws_size, hipStream_t stream) {
  (void)in_sizes; (void)n_in; (void)out_size; (void)d_ws; (void)ws_size;
  gqa_fwd<<<NB * HKV * 4, 512, 0, stream>>>((const __bf16*)d_in[0], (const __bf16*)d_in[1], (const __bf16*)d_in[2], (__bf16*)d_out);
}
```

```cpp
#include <hip/hip_runtime.h>
#include <hip/hip_bf16.h>

typedef __attribute__((ext_vector_type(8))) __bf16 bf16x8;
typedef __attribute__((ext_vector_type(2))) __bf16 bf16x2;
typedef __attribute__((ext_vector_type(4))) float f32x4;
typedef __attribute__((ext_vector_type(2))) float f32x2;
typedef __attribute__((ext_vector_type(4))) short s16x4;
typedef __attribute__((ext_vector_type(8))) short s16x8;
typedef __attribute__((ext_vector_type(4))) unsigned u32x4;
typedef __attribute__((ext_vector_type(2))) unsigned u32x2;
#define LDS_AS __attribute__((address_space(3)))
#define DI __device__ __forceinline__

constexpr int NB = 8, HQ = 16, HKV = 8, SQ = 144, SK = 1024, DH = 256;
constexpr int NRT = 9;
constexpr int QRT = 5;
constexpr int KT = 32;
constexpr int NT = SK / 2 / KT;
constexpr int ROWB = DH * 2;

constexpr int KSUB = 2 * 4096 + 64;
constexpr int KSLOT = 2 * KSUB;
constexpr int VSUB = 4 * 1056;
constexpr int VSLOT = 4 * VSUB;
constexpr int PBUF = NRT * 1024;
constexpr int OFF_K = 0;
constexpr int OFF_V = OFF_K + 4 * KSLOT;
constexpr int OFF_P = OFF_V + 4 * VSLOT;
constexpr int OFF_MX = OFF_P + 2 * PBUF;
constexpr int OFF_L = OFF_MX + 2 * SQ * 4;
constexpr int OFF_END = OFF_L + SQ * 2 * 4;
constexpr int LDS_BYTES = OFF_END;
#define STAMP() do {} while (0)
#define STAMP_DUMP() do {} while (0)
constexpr int OFF_Q = OFF_K + 3 * KSLOT;
static_assert(OFF_Q + 8 * KSUB <= OFF_V + 3 * VSLOT, "Q staging too large");
static_assert(LDS_BYTES <= 163840, "LDS");
constexpr int WS_PAY = 256 * 4 * 128;
constexpr int PAYB = 10 * 1024;

constexpr float SC_LOG2E = 0.0625f * 1.4426950408889634f;
constexpr float RESCALE_THR = 8.0f / SC_LOG2E;

#define POL "nt"
#define PRIO_PV 0
#define AB_NODMA 0
#define AB_NOSM 0
#define AB_NOAT 0
#define AB_NOPV 0
#ifndef NVA
#define NVA 3
#endif

DI unsigned rfl(unsigned x) { return __builtin_amdgcn_readfirstlane(x); }
DI void dma16(unsigned m0v, unsigned voff, __amdgpu_buffer_rsrc_t srd, unsigned soff) {
  asm volatile("s_mov_b32 m0, %0\n\ts_nop 0\n\tbuffer_load_dwordx4 %1, %2, %3 offen " POL " lds"
               :: "s"(m0v), "v"(voff), "s"(srd), "s"(soff) : "memory");
}
template <int N> DI void wait_vm() { asm volatile("s_waitcnt vmcnt(%0)" :: "i"(N) : "memory"); }
DI void wg_barrier() {
  asm volatile("s_waitcnt lgkmcnt(0)\n\ts_barrier" ::: "memory");
}
#define SB0() __builtin_amdgcn_sched_barrier(0)
DI float fmax2(float a, float b) { return __builtin_elementwise_maximum(a, b); }
DI unsigned pk2(float a, float b) {
  f32x2 v = {a, b};
  return __builtin_bit_cast(unsigned, __builtin_convertvector(v, bf16x2));
}
DI f32x4 mfma16(bf16x8 a, bf16x8 b, f32x4 c) { return __builtin_amdgcn_mfma_f32_16x16x32_bf16(a, b, c, 0, 0, 0); }
DI unsigned ld_dev(const unsigned* p) { return __hip_atomic_load(p, __ATOMIC_RELAXED, __HIP_MEMORY_SCOPE_AGENT); }
DI void st_dev(unsigned* p, unsigned v) { __hip_atomic_store(p, v, __ATOMIC_RELAXED, __HIP_MEMORY_SCOPE_AGENT); }

__global__ __launch_bounds__(512, 2) void gqa_fwd(const __bf16* __restrict__ Q, const __bf16* __restrict__ K,
                                                  const __bf16* __restrict__ V, __bf16* __restrict__ O, char* WS) {
  __shared__ __attribute__((aligned(16))) char smem[LDS_BYTES];
  LDS_AS char* lds = (LDS_AS char*)smem;
  const unsigned lds0 = (unsigned)(size_t)lds;
  const int tid = threadIdx.x;
  const int lane = tid & 63;
  const int wave = rfl(tid >> 6);
  const int w = wave & 3;
  const int r16 = lane & 15, g = lane >> 4;
  int stn = 0; (void)stn;
  STAMP();
  const int bid = blockIdx.x;
  const int bh = (bid & 7) * 8 + (bid >> 5);
  const int qh = (bid >> 3) & 1, kh = (bid >> 4) & 1;
  const int row0 = (bh * 2 + qh) * SQ;
  const size_t kv0 = ((size_t)bh * SK + kh * (SK / 2)) * DH;

  const __amdgpu_buffer_rsrc_t srdQ = __builtin_amdgcn_make_buffer_rsrc((void*)(Q + (size_t)row0 * DH), (short)0, SQ * ROWB, 0x00020000);
  const __amdgpu_buffer_rsrc_t srdK = __builtin_amdgcn_make_buffer_rsrc((void*)(K + kv0), (short)0, SK / 2 * ROWB, 0x00020000);
  const __amdgpu_buffer_rsrc_t srdV = __builtin_amdgcn_make_buffer_rsrc((void*)(V + kv0), (short)0, SK / 2 * ROWB, 0x00020000);

  unsigned* const fl_own = (unsigned*)(WS + (bid * 4 + w) * 128);
  unsigned* const fl_oth = (unsigned*)(WS + ((bid ^ 16) * 4 + w) * 128);

  const unsigned vsrc_row = (lane >> 3) * ROWB + (((lane & 7) ^ ((lane >> 4) & 3)) << 4);
  if (tid < 2 * SQ) *(LDS_AS float*)(lds + OFF_MX + tid * 4) = -INFINITY;

  asm volatile("s_nop 4" ::: "memory");
#pragma unroll
  for (int i = 0; i < 2; ++i) {
    const int pc = wave * 2 + i, rb = pc >> 2, cb = pc & 3;
    dma16(rfl(lds0 + OFF_K + (rb >> 1) * KSUB + (rb & 1) * 4160 + cb * 1024), vsrc_row, srdK, rfl(rb * 8 * ROWB + cb * 128));
  }
#pragma unroll
  for (int i = 0; i < 9; ++i) {
    const int rb = i * 2 + (wave >> 2), cb = w;
    dma16(rfl(lds0 + (i < 8 ? OFF_Q + i * KSUB : OFF_P) + (rb & 1) * 4160 + cb * 1024), vsrc_row, srdQ, rfl(rb * 8 * ROWB + cb * 128));
  }

  if (wave < 4) {
    const int rh = w & 1, kg = w >> 1;
    wait_vm<0>();
    wg_barrier();
    STAMP();
    const int frag_off = (r16 >> 3) * 4160 + (r16 & 7) * 128 + ((g ^ ((r16 >> 1) & 3)) << 4);
    bf16x8 qf[QRT][8];
    {
      LDS_AS char* qa = lds + OFF_Q + rh * 4 * KSUB + frag_off;
      LDS_AS char* qb = lds + (rh ? OFF_P : OFF_Q + 4 * KSUB) + frag_off;
#pragma unroll
      for (int s = 0; s < 8; ++s)
#pragma unroll
        for (int rt = 0; rt < QRT; ++rt)
          qf[rt][s] = *(LDS_AS bf16x8*)((rt < 4 ? qa + rt * KSUB : qb) + (s >> 1) * 1024 + (s & 1) * 64);
    }
    LDS_AS char* kp = lds + OFF_K + kg * KSUB + frag_off;
    LDS_AS char* pp = lds + OFF_P + rh * 4096 + lane * 16 + kg * 8;
    LDS_AS float* mxp = (LDS_AS float*)(lds + OFF_MX) + rh * 64 + r16;
    float mthr[QRT], mcs[QRT], lsum[QRT], mx[QRT];
    f32x4 sacc[2][QRT];
    bf16x8 kf0, kf1;
#pragma unroll
    for (int rt = 0; rt < QRT; ++rt) { mthr[rt] = -INFINITY; mcs[rt] = INFINITY; lsum[rt] = 0.f; }

    auto mxread = [&](int par) {
#pragma unroll
      for (int rt = 0; rt < QRT; ++rt) mx[rt] = mxp[par * SQ + rt * 16];
    };
    auto check = [&]() {
      bool grow = false;
#pragma unroll
      for (int rt = 0; rt < QRT; ++rt) grow |= mx[rt] > mthr[rt];
      if (__builtin_amdgcn_ballot_w64(grow)) {
#pragma unroll
        for (int rt = 0; rt < QRT; ++rt) {
          const bool gr = mx[rt] > mthr[rt];
          const float mcn = gr ? -mx[rt] * SC_LOG2E : mcs[rt];
          lsum[rt] *= __builtin_amdgcn_exp2f(mcn - mcs[rt]);
          mcs[rt] = mcn;
          mthr[rt] = gr ? mx[rt] + RESCALE_THR : mthr[rt];
        }
      }
    };
    auto sm_rt = [&](int par, int rt) {
      const float mc = mcs[rt];
      float p[4];
#pragma unroll
      for (int i = 0; i < 4; ++i) p[i] = __builtin_amdgcn_exp2f(__builtin_fmaf(sacc[par][rt][i], SC_LOG2E, mc));
      float a = p[0] + p[1], b = p[2] + p[3];
      asm volatile("" : "+v"(a));
      asm volatile("" : "+v"(b));
      a += b;
      asm volatile("" : "+v"(a));
      lsum[rt] += a;
      asm volatile("" : "+v"(lsum[rt]));
      u32x2 pk = {pk2(p[0], p[1]), pk2(p[2], p[3])};
      *(LDS_AS u32x2*)(pp + par * PBUF + rt * 1024) = pk;
    };
    auto kread = [&](LDS_AS char* b, int s) { return *(LDS_AS bf16x8*)(b + (s >> 1) * 1024 + (s & 1) * 64); };
    auto iter = [&](int t, int par, bool sm, bool next) {
      LDS_AS char* kc = kp + (t & 3) * KSLOT;
      LDS_AS char* kn = kp + ((t + 1) & 3) * KSLOT;
      bf16x8 kf[8];
      kf[0] = kf0; kf[1] = kf1;
      if (sm) mxread(par ^ 1);
#pragma unroll
      for (int s = 0; s < 8; ++s) {
        if (s + 2 < 8) kf[s + 2] = kread(kc, s + 2);
        if (next && s == 6) kf0 = kread(kn, 0);
        if (next && s == 7) kf1 = kread(kn, 1);
#pragma unroll
        for (int rt = 0; rt < QRT; ++rt) sacc[par][rt] = mfma16(kf[s], qf[rt][s], s == 0 ? f32x4{0.f, 0.f, 0.f, 0.f} : sacc[par][rt]);
        if (sm && s == 1) { SB0(); check(); }
        if (!AB_NOSM && sm && s >= 2 && s <= 6) {
          sm_rt(par ^ 1, s - 2);
#pragma unroll
          for (int i = 0; i < 5; ++i) {
            __builtin_amdgcn_sched_group_barrier(0x008, 1, 0);
            __builtin_amdgcn_sched_group_barrier(0x002, NVA, 0);
          }
        }
        SB0();
      }
#pragma unroll
      for (int rt = 0; rt < QRT; ++rt) {
        const float m = fmax2(fmax2(sacc[par][rt][0], sacc[par][rt][1]), fmax2(sacc[par][rt][2], sacc[par][rt][3]));
        if (AB_NOAT) asm volatile("" :: "v"(m)); else __hip_atomic_fetch_max(mxp + par * SQ + rt * 16, m, __ATOMIC_RELAXED, __HIP_MEMORY_SCOPE_WORKGROUP);
      }
    };
    kf0 = kread(kp, 0); kf1 = kread(kp, 1);
    wg_barrier();
    STAMP();
    iter(0, 0, false, true);
    STAMP();
    wg_barrier();
#pragma unroll 1
    for (int t = 1; t < NT - 1; t += 2) {
      STAMP();
      iter(t, 1, true, true);
      STAMP();
      wg_barrier();
      STAMP();
      iter(t + 1, 0, true, true);
      STAMP();
      wg_barrier();
    }
    iter(NT - 1, 1, true, false);
    wg_barrier();
    mxread(1);
    check();
#pragma unroll
    for (int rt = 0; rt < QRT; ++rt) sm_rt(1, rt);
#pragma unroll
    for (int rt = 0; rt < QRT; ++rt) {
      const unsigned u0 = __builtin_bit_cast(unsigned, lsum[rt]);
      const auto x = __builtin_amdgcn_permlane16_swap(u0, u0, false, false);
      const unsigned x0 = x[0], x1 = x[1];
      const unsigned u1 = __builtin_bit_cast(unsigned, __builtin_bit_cast(float, x0) + __builtin_bit_cast(float, x1));
      const auto y = __builtin_amdgcn_permlane32_swap(u1, u1, false, false);
      const unsigned y0 = y[0], y1 = y[1];
      *(LDS_AS float*)(lds + OFF_L + (((rh * 4 + rt) * 16 + r16) * 2 + kg) * 4) = __builtin_bit_cast(float, y0) + __builtin_bit_cast(float, y1);
    }
    wg_barrier();
    STAMP();
    STAMP_DUMP();
  } else {
    const unsigned vsrc_v = (16 * (lane >> 5) + 8 * ((lane >> 4) & 1) + ((lane >> 3) & 1)) * ROWB + (lane & 7) * 16 + w * 128;
    const unsigned kdst = lds0 + OFF_K + (w >> 1) * KSUB + (w & 1) * 4160;
    const unsigned vdst = lds0 + OFF_V + w * VSUB;
    auto dma_k = [&](int t, int cb) { dma16(kdst + (t & 3) * KSLOT + cb * 1024, vsrc_row, srdK, (t * KT + w * 8) * ROWB + cb * 128); };
    auto dma_v = [&](int t, int pc) { dma16(vdst + ((t + 3) & 3) * VSLOT + pc * 1056, vsrc_v, srdV, (t * KT + pc * 2) * ROWB); };
    unsigned nonce;
    asm volatile("global_load_dword %0, %1, off offset:64 sc1" : "=v"(nonce) : "v"(fl_own) : "memory");
#pragma unroll
    for (int pc = 0; pc < 4; ++pc) dma_k(1, pc);
#pragma unroll
    for (int pc = 0; pc < 4; ++pc) dma_v(0, pc);
#pragma unroll
    for (int pc = 0; pc < 4; ++pc) dma_k(2, pc);
    asm volatile("s_waitcnt vmcnt(12)" : "+v"(nonce) :: "memory");
    wg_barrier();
    STAMP();
    nonce += 1;
    asm volatile("global_store_dword %0, %1, off sc1" :: "v"(fl_own), "v"(nonce) : "memory");
    const int kk = 4 * g + (r16 >> 2), pq = lane & 3;
    LDS_AS char* vp = lds + OFF_V + w * VSUB + ((kk >> 1) & 3) * 1056 + (kk & 1) * 128 + (kk >> 3) * 256 + pq * 8;
    LDS_AS char* vpa = vp + kh * 64;
    LDS_AS char* vpb = vp + (kh ^ 1) * 64;
    LDS_AS char* pp = lds + OFF_P + lane * 16;
    LDS_AS float* mxp = (LDS_AS float*)(lds + OFF_MX) + r16;
    f32x4 oacc[4][NRT];
    float mthr[NRT], mcs[NRT];
#pragma unroll
    for (int rt = 0; rt < NRT; ++rt) {
      mthr[rt] = -INFINITY; mcs[rt] = INFINITY;
#pragma unroll
      for (int c = 0; c < 4; ++c) oacc[c][rt] = f32x4{0.f, 0.f, 0.f, 0.f};
    }
    bf16x8 vf[4];
    auto load_v = [&](int t, int c) {
      LDS_AS char* b = (c < 2 ? vpa : vpb) + ((t + 3) & 3) * VSLOT + (c & 1) * 32;
      const s16x4 x = __builtin_amdgcn_ds_read_tr16_b64_v4i16((LDS_AS s16x4*)b);
      const s16x4 y = __builtin_amdgcn_ds_read_tr16_b64_v4i16((LDS_AS s16x4*)(b + 512));
      vf[c] = __builtin_bit_cast(bf16x8, __builtin_shufflevector(x, y, 0, 1, 2, 3, 4, 5, 6, 7));
    };
    auto pv = [&](int i, bool mm, bool upd) {
      LDS_AS char* pc = pp + (i & 1) * PBUF;
      LDS_AS float* mc = mxp + ((i & 1) ^ 1) * SQ;
      bf16x8 pf[NRT];
      float mx[NRT];
      bool grow = false;
      if (mm) {
#pragma unroll
        for (int rt = 0; rt < NRT; ++rt) pf[rt] = *(LDS_AS bf16x8*)(pc + rt * 1024);
      }
      SB0();
#pragma unroll
      for (int cc = 0; cc < 4; ++cc) {
        const int c = cc ^ 2;
#pragma unroll
        for (int rt = 0; rt < NRT; ++rt) {
          if (mm && !AB_NOPV) oacc[c][rt] = mfma16(vf[c], pf[rt], oacc[c][rt]);
          if (upd && cc == 2) grow |= mx[rt] > mthr[rt];
          if (rt == 3 || rt == 7) {
            const int d = cc * 2 + (rt >> 2);
            SB0();
            if (AB_NODMA && i > 1) {} else
            if (d < 4) { if (i + 1 < NT) dma_v(i + 1, d); }
            else { if (i + 3 < NT) dma_k(i + 3, d - 4); }
            SB0();
          }
        }
        if (upd) load_v(i - 1, c);
        if (upd && cc == 0) {
#pragma unroll
          for (int rt = 0; rt < NRT; ++rt) mx[rt] = mc[rt * 16];
        }
        SB0();
      }
      if (upd && __builtin_amdgcn_ballot_w64(grow)) {
#pragma unroll
        for (int rt = 0; rt < NRT; ++rt) {
          const bool gr = mx[rt] > mthr[rt];
          const float mcn = gr ? -mx[rt] * SC_LOG2E : mcs[rt];
          const float al = __builtin_amdgcn_exp2f(mcn - mcs[rt]);
          mcs[rt] = mcn;
          mthr[rt] = gr ? mx[rt] + RESCALE_THR : mthr[rt];
#pragma unroll
          for (int c = 0; c < 4; ++c) oacc[c][rt] *= al;
        }
      }
    };
    wait_vm<8>();
    wg_barrier();
    pv(0, false, false);
    wait_vm<8>();
    wg_barrier();
    pv(1, false, true);
    wait_vm<8>();
    wg_barrier();
    if (PRIO_PV) __builtin_amdgcn_s_setprio(PRIO_PV);
#pragma unroll 1
    for (int i = 2;; ++i) {
      STAMP();
      pv(i, true, true);
      if (i == NT + 1) break;
      if (i < NT - 3) wait_vm<8>(); else if (i < NT - 1) wait_vm<4>(); else wait_vm<0>();
      STAMP();
      wg_barrier();
    }
    unsigned echoed = ld_dev(fl_oth);
    float lse[12], inv[NRT];
    u32x4 tx[NRT];
#pragma unroll
    for (int rt = 0; rt < NRT; ++rt) {
      const f32x2 s2 = *(const LDS_AS f32x2*)(lds + OFF_L + (rt * 16 + r16) * 8);
      const float l = s2[0] + s2[1];
      inv[rt] = __builtin_amdgcn_rcpf(l);
      lse[rt] = __builtin_amdgcn_logf(l) - mcs[rt];
    }
    lse[9] = lse[10] = lse[11] = 0.f;
    STAMP();
    const __amdgpu_buffer_rsrc_t srdS = __builtin_amdgcn_make_buffer_rsrc(WS + WS_PAY + (size_t)(bid * 4 + w) * PAYB, (short)0, PAYB, 0x00020000);
    const __amdgpu_buffer_rsrc_t srdR = __builtin_amdgcn_make_buffer_rsrc(WS + WS_PAY + (size_t)((bid ^ 16) * 4 + w) * PAYB, (short)0, PAYB, 0x00020000);
#pragma unroll
    for (int rt = 0; rt < NRT; ++rt) {
      const f32x4 a = oacc[2][rt] * inv[rt], b = oacc[3][rt] * inv[rt];
      tx[rt] = u32x4{pk2(a[0], a[1]), pk2(a[2], a[3]), pk2(b[0], b[1]), pk2(b[2], b[3])};
    }
    SB0();
#pragma unroll
    for (int rt = 0; rt < NRT; ++rt) __builtin_amdgcn_raw_buffer_store_b128(tx[rt], srdS, lane * 16, rt * 1024, 16);
    asm volatile("s_nop 1" ::: "memory");
    SB0();
    if (g == 0) {
#pragma unroll
      for (int u = 0; u < 3; ++u) {
        const f32x4 x = {lse[4 * u], lse[4 * u + 1], lse[4 * u + 2], lse[4 * u + 3]};
        __builtin_amdgcn_raw_buffer_store_b128(__builtin_bit_cast(u32x4, x), srdS, r16 * 16, 9 * 1024 + u * 256, 16);
      }
    }
    SB0();
    wait_vm<0>();
    STAMP();
    st_dev(fl_oth + 16, echoed);
    while (true) {
      const unsigned a = ld_dev(fl_own + 16);
      const unsigned r = ld_dev(fl_oth);
      if (rfl(r) != rfl(echoed)) { echoed = r; st_dev(fl_oth + 16, r); }
      if (rfl(a) == rfl(nonce)) break;
    }
    asm volatile("s_waitcnt vmcnt(0)" ::: "memory");
    STAMP();
    const unsigned rlast = ld_dev(fl_oth);
    f32x4 ml[3];
    u32x4 rx[NRT];
#pragma unroll
    for (int u = 0; u < 3; ++u) ml[u] = __builtin_bit_cast(f32x4, __builtin_amdgcn_raw_buffer_load_b128(srdR, r16 * 16, 9 * 1024 + u * 256, 16));
#pragma unroll
    for (int rt = 0; rt < NRT; ++rt) rx[rt] = __builtin_amdgcn_raw_buffer_load_b128(srdR, lane * 16, rt * 1024, 16);
    if (rfl(rlast) != rfl(echoed)) st_dev(fl_oth + 16, rlast);
#pragma unroll
    for (int rt = 0; rt < NRT; ++rt) {
      const float lo = ml[rt >> 2][rt & 3];
      const float mm = fmaxf(lse[rt], lo);
      const float ea = __builtin_amdgcn_exp2f(lse[rt] - mm), eb = __builtin_amdgcn_exp2f(lo - mm);
      const float rs = __builtin_amdgcn_rcpf(ea + eb);
      const float fa = ea * rs * inv[rt], fb = eb * rs;
      f32x4 pa, pb;
#pragma unroll
      for (int j = 0; j < 2; ++j) {
        pa[2 * j] = __builtin_bit_cast(float, rx[rt][j] << 16); pa[2 * j + 1] = __builtin_bit_cast(float, rx[rt][j] & 0xffff0000u);
        pb[2 * j] = __builtin_bit_cast(float, rx[rt][2 + j] << 16); pb[2 * j + 1] = __builtin_bit_cast(float, rx[rt][2 + j] & 0xffff0000u);
      }
      const f32x4 a = oacc[0][rt] * fa + pa * fb, b = oacc[1][rt] * fa + pb * fb;
      const auto x = __builtin_amdgcn_permlane16_swap(pk2(a[0], a[1]), pk2(b[0], b[1]), false, false);
      const auto y = __builtin_amdgcn_permlane16_swap(pk2(a[2], a[3]), pk2(b[2], b[3]), false, false);
      const u32x4 o = {x[0], y[0], x[1], y[1]};
      *(u32x4*)(O + (size_t)(row0 + rt * 16 + r16) * DH + w * 64 + kh * 32 + (g & 1) * 16 + (g >> 1) * 8) = o;
    }
    STAMP();
    STAMP_DUMP();
  }
}

extern "C" void kernel_launch(void* const* d_in, const int* in_sizes, int n_in, void* d_out, int out_size,
                              void* d_ws, size_t ws_size, hipStream_t stream) {
  (void)in_sizes; (void)n_in; (void)out_size; (void)ws_size;
  gqa_fwd<<<NB * HQ * 2, 512, 0, stream>>>((const __bf16*)d_in[0], (const __bf16*)d_in[1], (const __bf16*)d_in[2], (__bf16*)d_out, (char*)d_ws);
}
```

```cpp
#include <hip/hip_runtime.h>
#include <hip/hip_bf16.h>

typedef __attribute__((ext_vector_type(8))) __bf16 bf16x8;
typedef __attribute__((ext_vector_type(2))) __bf16 bf16x2;
typedef __attribute__((ext_vector_type(4))) float f32x4;
typedef __attribute__((ext_vector_type(2))) float f32x2;
typedef __attribute__((ext_vector_type(4))) short s16x4;
typedef __attribute__((ext_vector_type(8))) short s16x8;
typedef __attribute__((ext_vector_type(4))) unsigned u32x4;
typedef __attribute__((ext_vector_type(2))) unsigned u32x2;
#define LDS_AS __attribute__((address_space(3)))
#define DI __device__ __forceinline__

constexpr int NB = 8, HQ = 16, HKV = 8, SQ = 144, SK = 1024, DH = 256;
constexpr int NRT = 9;
constexpr int QRT = 5;
constexpr int KT = 32;
constexpr int NT = SK / 2 / KT;
constexpr int ROWB = DH * 2;

constexpr int KSUB = 2 * 4096 + 64;
constexpr int KSLOT = 2 * KSUB;
constexpr int VSUB = 4 * 1056;
constexpr int VSLOT = 4 * VSUB;
constexpr int PBUF = NRT * 1024;
constexpr int OFF_K = 0;
constexpr int OFF_V = OFF_K + 4 * KSLOT;
constexpr int OFF_P = OFF_V + 4 * VSLOT;
constexpr int OFF_MX = OFF_P + 2 * PBUF;
constexpr int OFF_L = OFF_MX + 2 * SQ * 4;
constexpr int OFF_SINK = OFF_L + SQ * 2 * 4;
constexpr int OFF_END = OFF_SINK + 256;
constexpr int LDS_BYTES = OFF_END;
#define STAMP() do {} while (0)
#define STAMP_DUMP() do {} while (0)
constexpr int OFF_Q = OFF_K + 3 * KSLOT;
static_assert(OFF_Q + 8 * KSUB <= OFF_V + 3 * VSLOT, "Q staging too large");
static_assert(LDS_BYTES <= 163840, "LDS");
constexpr int WS_PAY = 256 * 4 * 128;
constexpr int PAYB = 10 * 1024;

constexpr float SC_LOG2E = 0.0625f * 1.4426950408889634f;
constexpr float RESCALE_THR = 8.0f / SC_LOG2E;

#define POL "nt"
#define PRIO_PV 0
#define AB_NODMA 0
#define AB_NOSM 0
#define AB_NOAT 0
#define AB_NOPV 0
#ifndef NVA
#define NVA 3
#endif

DI unsigned rfl(unsigned x) { return __builtin_amdgcn_readfirstlane(x); }
DI void dma16(unsigned m0v, unsigned voff, __amdgpu_buffer_rsrc_t srd, unsigned soff) {
  asm volatile("s_mov_b32 m0, %0\n\ts_nop 0\n\tbuffer_load_dwordx4 %1, %2, %3 offen " POL " lds"
               :: "s"(m0v), "v"(voff), "s"(srd), "s"(soff) : "memory");
}
template <int N> DI void wait_vm() { asm volatile("s_waitcnt vmcnt(%0)" :: "i"(N) : "memory"); }
DI void wg_barrier() {
  asm volatile("s_waitcnt lgkmcnt(0)\n\ts_barrier" ::: "memory");
}
#define SB0() __builtin_amdgcn_sched_barrier(0)
DI float fmax2(float a, float b) { return __builtin_elementwise_maximum(a, b); }
DI unsigned pk2(float a, float b) {
  f32x2 v = {a, b};
  return __builtin_bit_cast(unsigned, __builtin_convertvector(v, bf16x2));
}
DI f32x4 mfma16(bf16x8 a, bf16x8 b, f32x4 c) { return __builtin_amdgcn_mfma_f32_16x16x32_bf16(a, b, c, 0, 0, 0); }
DI unsigned ld_dev(const unsigned* p) { return __hip_atomic_load(p, __ATOMIC_RELAXED, __HIP_MEMORY_SCOPE_AGENT); }
DI void st_dev(unsigned* p, unsigned v) { __hip_atomic_store(p, v, __ATOMIC_RELAXED, __HIP_MEMORY_SCOPE_AGENT); }

__global__ __launch_bounds__(512, 2) void gqa_fwd(const __bf16* __restrict__ Q, const __bf16* __restrict__ K,
                                                  const __bf16* __restrict__ V, __bf16* __restrict__ O, char* WS) {
  __shared__ __attribute__((aligned(16))) char smem[LDS_BYTES];
  LDS_AS char* lds = (LDS_AS char*)smem;
  const unsigned lds0 = (unsigned)(size_t)lds;
  const int tid = threadIdx.x;
  const int lane = tid & 63;
  const int wave = rfl(tid >> 6);
  const int w = wave & 3;
  const int r16 = lane & 15, g = lane >> 4;
  int stn = 0; (void)stn;
  STAMP();
  const int bid = blockIdx.x;
  const int bh = (bid & 7) * 8 + (bid >> 5);
  const int qh = (bid >> 3) & 1, kh = (bid >> 4) & 1;
  const int row0 = (bh * 2 + qh) * SQ;
  const size_t kv0 = ((size_t)bh * SK + kh * (SK / 2)) * DH;

  const __amdgpu_buffer_rsrc_t srdQ = __builtin_amdgcn_make_buffer_rsrc((void*)(Q + (size_t)row0 * DH), (short)0, SQ * ROWB, 0x00020000);
  const __amdgpu_buffer_rsrc_t srdK = __builtin_amdgcn_make_buffer_rsrc((void*)(K + kv0), (short)0, SK / 2 * ROWB, 0x00020000);
  const __amdgpu_buffer_rsrc_t srdV = __builtin_amdgcn_make_buffer_rsrc((void*)(V + kv0), (short)0, SK / 2 * ROWB, 0x00020000);

  unsigned* const fl_own = (unsigned*)(WS + (bid * 4 + w) * 128);
  unsigned* const fl_oth = (unsigned*)(WS + ((bid ^ 16) * 4 + w) * 128);

  const unsigned vsrc_row = (lane >> 3) * ROWB + (((lane & 7) ^ ((lane >> 4) & 3)) << 4);
  if (tid < 2 * SQ) *(LDS_AS float*)(lds + OFF_MX + tid * 4) = -INFINITY;

  asm volatile("s_nop 4" ::: "memory");
#pragma unroll
  for (int i = 0; i < 2; ++i) {
    const int pc = wave * 2 + i, rb = pc >> 2, cb = pc & 3;
    dma16(rfl(lds0 + OFF_K + (rb >> 1) * KSUB + (rb & 1) * 4160 + cb * 1024), vsrc_row, srdK, rfl(rb * 8 * ROWB + cb * 128));
  }
#pragma unroll
  for (int i = 0; i < 9; ++i) {
    const int rb = i * 2 + (wave >> 2), cb = w;
    dma16(rfl(lds0 + (i < 8 ? OFF_Q + i * KSUB : OFF_P) + (rb & 1) * 4160 + cb * 1024), vsrc_row, srdQ, rfl(rb * 8 * ROWB + cb * 128));
  }

  if (wave < 4) {
    const int rh = w & 1, kg = w >> 1;
    {
      const char* tb = w == 0 ? (const char*)(K + kv0) : w == 1 ? (const char*)(V + kv0) : w == 2 ? (const char*)(O + (size_t)row0 * DH) : WS + WS_PAY;
      const int tn = w < 2 ? SK / 2 * ROWB : w == 2 ? SQ * ROWB : 256 * 4 * PAYB;
      const __amdgpu_buffer_rsrc_t srdT = __builtin_amdgcn_make_buffer_rsrc((void*)tb, (short)0, tn, 0x00020000);
      const unsigned to = w < 3 ? lane * 4096 : ((lane & 16) ? bid ^ 16 : bid) * 4 * PAYB + ((lane & 15) >= 10 ? 0 : (lane & 15) * 4096);
      asm volatile("s_mov_b32 m0, %0\n\ts_nop 4\n\tbuffer_load_dword %1, %2, 0 offen lds" :: "s"(lds0 + OFF_SINK), "v"(to), "s"(srdT) : "memory");
    }
    wait_vm<1>();
    wg_barrier();
    STAMP();
    const int frag_off = (r16 >> 3) * 4160 + (r16 & 7) * 128 + ((g ^ ((r16 >> 1) & 3)) << 4);
    bf16x8 qf[QRT][8];
    {
      LDS_AS char* qa = lds + OFF_Q + rh * 4 * KSUB + frag_off;
      LDS_AS char* qb = lds + (rh ? OFF_P : OFF_Q + 4 * KSUB) + frag_off;
#pragma unroll
      for (int s = 0; s < 8; ++s)
#pragma unroll
        for (int rt = 0; rt < QRT; ++rt)
          qf[rt][s] = *(LDS_AS bf16x8*)((rt < 4 ? qa + rt * KSUB : qb) + (s >> 1) * 1024 + (s & 1) * 64);
    }
    LDS_AS char* kp = lds + OFF_K + kg * KSUB + frag_off;
    LDS_AS char* pp = lds + OFF_P + rh * 4096 + lane * 16 + kg * 8;
    LDS_AS float* mxp = (LDS_AS float*)(lds + OFF_MX) + rh * 64 + r16;
    float mthr[QRT], mcs[QRT], lsum[QRT], mx[QRT];
    f32x4 sacc[2][QRT];
    bf16x8 kf0, kf1;
#pragma unroll
    for (int rt = 0; rt < QRT; ++rt) { mthr[rt] = -INFINITY; mcs[rt] = INFINITY; lsum[rt] = 0.f; }

    auto mxread = [&](int par) {
#pragma unroll
      for (int rt = 0; rt < QRT; ++rt) mx[rt] = mxp[par * SQ + rt * 16];
    };
    auto check = [&]() {
      bool grow = false;
#pragma unroll
      for (int rt = 0; rt < QRT; ++rt) grow |= mx[rt] > mthr[rt];
      if (__builtin_amdgcn_ballot_w64(grow)) {
#pragma unroll
        for (int rt = 0; rt < QRT; ++rt) {
          const bool gr = mx[rt] > mthr[rt];
          const float mcn = gr ? -mx[rt] * SC_LOG2E : mcs[rt];
          lsum[rt] *= __builtin_amdgcn_exp2f(mcn - mcs[rt]);
          mcs[rt] = mcn;
          mthr[rt] = gr ? mx[rt] + RESCALE_THR : mthr[rt];
        }
      }
    };
    auto sm_rt = [&](int par, int rt) {
      const float mc = mcs[rt];
      float p[4];
#pragma unroll
      for (int i = 0; i < 4; ++i) p[i] = __builtin_amdgcn_exp2f(__builtin_fmaf(sacc[par][rt][i], SC_LOG2E, mc));
      float a = p[0] + p[1], b = p[2] + p[3];
      asm volatile("" : "+v"(a));
      asm volatile("" : "+v"(b));
      a += b;
      asm volatile("" : "+v"(a));
      lsum[rt] += a;
      asm volatile("" : "+v"(lsum[rt]));
      u32x2 pk = {pk2(p[0], p[1]), pk2(p[2], p[3])};
      *(LDS_AS u32x2*)(pp + par * PBUF + rt * 1024) = pk;
    };
    auto kread = [&](LDS_AS char* b, int s) { return *(LDS_AS bf16x8*)(b + (s >> 1) * 1024 + (s & 1) * 64); };
    auto iter = [&](int t, int par, bool sm, bool next) {
      LDS_AS char* kc = kp + (t & 3) * KSLOT;
      LDS_AS char* kn = kp + ((t + 1) & 3) * KSLOT;
      bf16x8 kf[8];
      kf[0] = kf0; kf[1] = kf1;
      if (sm) mxread(par ^ 1);
#pragma unroll
      for (int s = 0; s < 8; ++s) {
        if (s + 2 < 8) kf[s + 2] = kread(kc, s + 2);
        if (next && s == 6) kf0 = kread(kn, 0);
        if (next && s == 7) kf1 = kread(kn, 1);
#pragma unroll
        for (int rt = 0; rt < QRT; ++rt) sacc[par][rt] = mfma16(kf[s], qf[rt][s], s == 0 ? f32x4{0.f, 0.f, 0.f, 0.f} : sacc[par][rt]);
        if (sm && s == 1) { SB0(); check(); }
        if (!AB_NOSM && sm && s >= 2 && s <= 6) {
          sm_rt(par ^ 1, s - 2);
#pragma unroll
          for (int i = 0; i < 5; ++i) {
            __builtin_amdgcn_sched_group_barrier(0x008, 1, 0);
            __builtin_amdgcn_sched_group_barrier(0x002, NVA, 0);
          }
        }
        SB0();
      }
#pragma unroll
      for (int rt = 0; rt < QRT; ++rt) {
        const float m = fmax2(fmax2(sacc[par][rt][0], sacc[par][rt][1]), fmax2(sacc[par][rt][2], sacc[par][rt][3]));
        if (AB_NOAT) asm volatile("" :: "v"(m)); else __hip_atomic_fetch_max(mxp + par * SQ + rt * 16, m, __ATOMIC_RELAXED, __HIP_MEMORY_SCOPE_WORKGROUP);
      }
    };
    kf0 = kread(kp, 0); kf1 = kread(kp, 1);
    wg_barrier();
    STAMP();
    iter(0, 0, false, true);
    STAMP();
    wg_barrier();
#pragma unroll 1
    for (int t = 1; t < NT - 1; t += 2) {
      STAMP();
      iter(t, 1, true, true);
      STAMP();
      wg_barrier();
      STAMP();
      iter(t + 1, 0, true, true);
      STAMP();
      wg_barrier();
    }
    iter(NT - 1, 1, true, false);
    wg_barrier();
    mxread(1);
    check();
#pragma unroll
    for (int rt = 0; rt < QRT; ++rt) sm_rt(1, rt);
#pragma unroll
    for (int rt = 0; rt < QRT; ++rt) {
      const unsigned u0 = __builtin_bit_cast(unsigned, lsum[rt]);
      const auto x = __builtin_amdgcn_permlane16_swap(u0, u0, false, false);
      const unsigned x0 = x[0], x1 = x[1];
      const unsigned u1 = __builtin_bit_cast(unsigned, __builtin_bit_cast(float, x0) + __builtin_bit_cast(float, x1));
      const auto y = __builtin_amdgcn_permlane32_swap(u1, u1, false, false);
      const unsigned y0 = y[0], y1 = y[1];
      *(LDS_AS float*)(lds + OFF_L + (((rh * 4 + rt) * 16 + r16) * 2 + kg) * 4) = __builtin_bit_cast(float, y0) + __builtin_bit_cast(float, y1);
    }
    wg_barrier();
    STAMP();
    STAMP_DUMP();
  } else {
    const unsigned vsrc_v = (16 * (lane >> 5) + 8 * ((lane >> 4) & 1) + ((lane >> 3) & 1)) * ROWB + (lane & 7) * 16 + w * 128;
    const unsigned kdst = lds0 + OFF_K + (w >> 1) * KSUB + (w & 1) * 4160;
    const unsigned vdst = lds0 + OFF_V + w * VSUB;
    auto dma_k = [&](int t, int cb) { dma16(kdst + (t & 3) * KSLOT + cb * 1024, vsrc_row, srdK, (t * KT + w * 8) * ROWB + cb * 128); };
    auto dma_v = [&](int t, int pc) { dma16(vdst + ((t + 3) & 3) * VSLOT + pc * 1056, vsrc_v, srdV, (t * KT + pc * 2) * ROWB); };
    unsigned nonce;
    asm volatile("global_load_dword %0, %1, off offset:64 sc1" : "=v"(nonce) : "v"(fl_own) : "memory");
#pragma unroll
    for (int pc = 0; pc < 4; ++pc) dma_k(1, pc);
#pragma unroll
    for (int pc = 0; pc < 4; ++pc) dma_v(0, pc);
#pragma unroll
    for (int pc = 0; pc < 4; ++pc) dma_k(2, pc);
    asm volatile("s_waitcnt vmcnt(12)" : "+v"(nonce) :: "memory");
    wg_barrier();
    STAMP();
    nonce += 1;
    asm volatile("global_store_dword %0, %1, off sc1" :: "v"(fl_own), "v"(nonce) : "memory");
    const int kk = 4 * g + (r16 >> 2), pq = lane & 3;
    LDS_AS char* vp = lds + OFF_V + w * VSUB + ((kk >> 1) & 3) * 1056 + (kk & 1) * 128 + (kk >> 3) * 256 + pq * 8;
    LDS_AS char* vpa = vp + kh * 64;
    LDS_AS char* vpb = vp + (kh ^ 1) * 64;
    LDS_AS char* pp = lds + OFF_P + lane * 16;
    LDS_AS float* mxp = (LDS_AS float*)(lds + OFF_MX) + r16;
    f32x4 oacc[4][NRT];
    float mthr[NRT], mcs[NRT];
#pragma unroll
    for (int rt = 0; rt < NRT; ++rt) {
      mthr[rt] = -INFINITY; mcs[rt] = INFINITY;
#pragma unroll
      for (int c = 0; c < 4; ++c) oacc[c][rt] = f32x4{0.f, 0.f, 0.f, 0.f};
    }
    bf16x8 vf[4];
    auto load_v = [&](int t, int c) {
      LDS_AS char* b = (c < 2 ? vpa : vpb) + ((t + 3) & 3) * VSLOT + (c & 1) * 32;
      const s16x4 x = __builtin_amdgcn_ds_read_tr16_b64_v4i16((LDS_AS s16x4*)b);
      const s16x4 y = __builtin_amdgcn_ds_read_tr16_b64_v4i16((LDS_AS s16x4*)(b + 512));
      vf[c] = __builtin_bit_cast(bf16x8, __builtin_shufflevector(x, y, 0, 1, 2, 3, 4, 5, 6, 7));
    };
    auto pv = [&](int i, bool mm, bool upd) {
      LDS_AS char* pc = pp + (i & 1) * PBUF;
      LDS_AS float* mc = mxp + ((i & 1) ^ 1) * SQ;
      bf16x8 pf[NRT];
      float mx[NRT];
      bool grow = false;
      if (mm) {
#pragma unroll
        for (int rt = 0; rt < NRT; ++rt) pf[rt] = *(LDS_AS bf16x8*)(pc + rt * 1024);
      }
      SB0();
#pragma unroll
      for (int cc = 0; cc < 4; ++cc) {
        const int c = cc ^ 2;
#pragma unroll
        for (int rt = 0; rt < NRT; ++rt) {
          if (mm && !AB_NOPV) oacc[c][rt] = mfma16(vf[c], pf[rt], oacc[c][rt]);
          if (upd && cc == 2) grow |= mx[rt] > mthr[rt];
          if (rt == 3 || rt == 7) {
            const int d = cc * 2 + (rt >> 2);
            SB0();
            if (AB_NODMA && i > 1) {} else
            if (d < 4) { if (i + 1 < NT) dma_v(i + 1, d); }
            else { if (i + 3 < NT) dma_k(i + 3, d - 4); }
            SB0();
          }
        }
        if (upd) load_v(i - 1, c);
        if (upd && cc == 0) {
#pragma unroll
          for (int rt = 0; rt < NRT; ++rt) mx[rt] = mc[rt * 16];
        }
        SB0();
      }
      if (upd && __builtin_amdgcn_ballot_w64(grow)) {
#pragma unroll
        for (int rt = 0; rt < NRT; ++rt) {
          const bool gr = mx[rt] > mthr[rt];
          const float mcn = gr ? -mx[rt] * SC_LOG2E : mcs[rt];
          const float al = __builtin_amdgcn_exp2f(mcn - mcs[rt]);
          mcs[rt] = mcn;
          mthr[rt] = gr ? mx[rt] + RESCALE_THR : mthr[rt];
#pragma unroll
          for (int c = 0; c < 4; ++c) oacc[c][rt] *= al;
        }
      }
    };
    wait_vm<8>();
    wg_barrier();
    pv(0, false, false);
    wait_vm<8>();
    wg_barrier();
    pv(1, false, true);
    wait_vm<8>();
    wg_barrier();
    if (PRIO_PV) __builtin_amdgcn_s_setprio(PRIO_PV);
#pragma unroll 1
    for (int i = 2;; ++i) {
      STAMP();
      pv(i, true, true);
      if (i == NT + 1) break;
      if (i < NT - 3) wait_vm<8>(); else if (i < NT - 1) wait_vm<4>(); else wait_vm<0>();
      STAMP();
      wg_barrier();
    }
    unsigned echoed = ld_dev(fl_oth);
    float lse[12], inv[NRT];
    u32x4 tx[NRT];
#pragma unroll
    for (int rt = 0; rt < NRT; ++rt) {
      const f32x2 s2 = *(const LDS_AS f32x2*)(lds + OFF_L + (rt * 16 + r16) * 8);
      const float l = s2[0] + s2[1];
      inv[rt] = __builtin_amdgcn_rcpf(l);
      lse[rt] = __builtin_amdgcn_logf(l) - mcs[rt];
    }
    lse[9] = lse[10] = lse[11] = 0.f;
    STAMP();
    const __amdgpu_buffer_rsrc_t srdS = __builtin_amdgcn_make_buffer_rsrc(WS + WS_PAY + (size_t)(bid * 4 + w) * PAYB, (short)0, PAYB, 0x00020000);
    const __amdgpu_buffer_rsrc_t srdR = __builtin_amdgcn_make_buffer_rsrc(WS + WS_PAY + (size_t)((bid ^ 16) * 4 + w) * PAYB, (short)0, PAYB, 0x00020000);
#pragma unroll
    for (int rt = 0; rt < NRT; ++rt) {
      const f32x4 a = oacc[2][rt] * inv[rt], b = oacc[3][rt] * inv[rt];
      tx[rt] = u32x4{pk2(a[0], a[1]), pk2(a[2], a[3]), pk2(b[0], b[1]), pk2(b[2], b[3])};
    }
    SB0();
#pragma unroll
    for (int rt = 0; rt < NRT; ++rt) __builtin_amdgcn_raw_buffer_store_b128(tx[rt], srdS, lane * 16, rt * 1024, 16);
    asm volatile("s_nop 1" ::: "memory");
    SB0();
    if (g == 0) {
#pragma unroll
      for (int u = 0; u < 3; ++u) {
        const f32x4 x = {lse[4 * u], lse[4 * u + 1], lse[4 * u + 2], lse[4 * u + 3]};
        __builtin_amdgcn_raw_buffer_store_b128(__builtin_bit_cast(u32x4, x), srdS, r16 * 16, 9 * 1024 + u * 256, 16);
      }
    }
    SB0();
    wait_vm<0>();
    STAMP();
    st_dev(fl_oth + 16, echoed);
    while (true) {
      const unsigned a = ld_dev(fl_own + 16);
      const unsigned r = ld_dev(fl_oth);
      if (rfl(r) != rfl(echoed)) { echoed = r; st_dev(fl_oth + 16, r); }
      if (rfl(a) == rfl(nonce)) break;
    }
    asm volatile("s_waitcnt vmcnt(0)" ::: "memory");
    STAMP();
    const unsigned rlast = ld_dev(fl_oth);
    f32x4 ml[3];
    u32x4 rx[NRT];
#pragma unroll
    for (int u = 0; u < 3; ++u) ml[u] = __builtin_bit_cast(f32x4, __builtin_amdgcn_raw_buffer_load_b128(srdR, r16 * 16, 9 * 1024 + u * 256, 16));
#pragma unroll
    for (int rt = 0; rt < NRT; ++rt) rx[rt] = __builtin_amdgcn_raw_buffer_load_b128(srdR, lane * 16, rt * 1024, 16);
    if (rfl(rlast) != rfl(echoed)) st_dev(fl_oth + 16, rlast);
#pragma unroll
    for (int rt = 0; rt < NRT; ++rt) {
      const float lo = ml[rt >> 2][rt & 3];
      const float mm = fmaxf(lse[rt], lo);
      const float ea = __builtin_amdgcn_exp2f(lse[rt] - mm), eb = __builtin_amdgcn_exp2f(lo - mm);
      const float rs = __builtin_amdgcn_rcpf(ea + eb);
      const float fa = ea * rs * inv[rt], fb = eb * rs;
      f32x4 pa, pb;
#pragma unroll
      for (int j = 0; j < 2; ++j) {
        pa[2 * j] = __builtin_bit_cast(float, rx[rt][j] << 16); pa[2 * j + 1] = __builtin_bit_cast(float, rx[rt][j] & 0xffff0000u);
        pb[2 * j] = __builtin_bit_cast(float, rx[rt][2 + j] << 16); pb[2 * j + 1] = __builtin_bit_cast(float, rx[rt][2 + j] & 0xffff0000u);
      }
      const f32x4 a = oacc[0][rt] * fa + pa * fb, b = oacc[1][rt] * fa + pb * fb;
      const auto x = __builtin_amdgcn_permlane16_swap(pk2(a[0], a[1]), pk2(b[0], b[1]), false, false);
      const auto y = __builtin_amdgcn_permlane16_swap(pk2(a[2], a[3]), pk2(b[2], b[3]), false, false);
      const u32x4 o = {x[0], y[0], x[1], y[1]};
      *(u32x4*)(O + (size_t)(row0 + rt * 16 + r16) * DH + w * 64 + kh * 32 + (g & 1) * 16 + (g >> 1) * 8) = o;
    }
    STAMP();
    STAMP_DUMP();
  }
}

extern "C" void kernel_launch(void* const* d_in, const int* in_sizes, int n_in, void* d_out, int out_size,
                              void* d_ws, size_t ws_size, hipStream_t stream) {
  (void)in_sizes; (void)n_in; (void)out_size; (void)ws_size;
  gqa_fwd<<<NB * HQ * 2, 512, 0, stream>>>((const __bf16*)d_in[0], (const __bf16*)d_in[1], (const __bf16*)d_in[2], (__bf16*)d_out, (char*)d_ws);
}
```

```cpp
#include <hip/hip_runtime.h>
#include <hip/hip_bf16.h>

typedef __attribute__((ext_vector_type(8))) __bf16 bf16x8;
typedef __attribute__((ext_vector_type(2))) __bf16 bf16x2;
typedef __attribute__((ext_vector_type(4))) float f32x4;
typedef __attribute__((ext_vector_type(2))) float f32x2;
typedef __attribute__((ext_vector_type(4))) short s16x4;
typedef __attribute__((ext_vector_type(8))) short s16x8;
typedef __attribute__((ext_vector_type(4))) unsigned u32x4;
typedef __attribute__((ext_vector_type(2))) unsigned u32x2;
#define LDS_AS __attribute__((address_space(3)))
#define DI __device__ __forceinline__

constexpr int NB = 8, HQ = 16, HKV = 8, SQ = 144, SK = 1024, DH = 256;
constexpr int NRT = 9;
constexpr int QRT = 5;
constexpr int KT = 32;
constexpr int NT = SK / 2 / KT;
constexpr int ROWB = DH * 2;

constexpr int KSUB = 2 * 4096 + 64;
constexpr int KSLOT = 2 * KSUB;
constexpr int VSUB = 4 * 1056;
constexpr int VSLOT = 4 * VSUB;
constexpr int PBUF = NRT * 1024;
constexpr int OFF_K = 0;
constexpr int OFF_V = OFF_K + 4 * KSLOT;
constexpr int OFF_P = OFF_V + 4 * VSLOT;
constexpr int OFF_MX = OFF_P + 2 * PBUF;
constexpr int OFF_L = OFF_MX + 2 * SQ * 4;
constexpr int OFF_GS = OFF_L + SQ * 2 * 4;
constexpr int OFF_END = OFF_GS + 16;
constexpr int LDS_BYTES = OFF_END;
#define STAMP() do {} while (0)
#define STAMP_DUMP() do {} while (0)
constexpr int OFF_Q = OFF_K + 3 * KSLOT;
static_assert(OFF_Q + 8 * KSUB <= OFF_V + 3 * VSLOT, "Q staging too large");
static_assert(LDS_BYTES <= 163840, "LDS");
constexpr int WS_PAY = 256 * 4 * 128;
constexpr int PAYB = 10 * 1024;

constexpr float SC_LOG2E = 0.0625f * 1.4426950408889634f;
constexpr float RESCALE_THR = 8.0f / SC_LOG2E;

#define POL "nt"
#define PRIO_PV 0
#define AB_NODMA 0
#define AB_NOSM 0
#define AB_NOAT 0
#define AB_NOPV 0
#ifndef NVA
#define NVA 3
#endif

DI unsigned rfl(unsigned x) { return __builtin_amdgcn_readfirstlane(x); }
DI void dma16(unsigned m0v, unsigned voff, __amdgpu_buffer_rsrc_t srd, unsigned soff) {
  asm volatile("s_mov_b32 m0, %0\n\ts_nop 0\n\tbuffer_load_dwordx4 %1, %2, %3 offen " POL " lds"
               :: "s"(m0v), "v"(voff), "s"(srd), "s"(soff) : "memory");
}
template <int N> DI void wait_vm() { asm volatile("s_waitcnt vmcnt(%0)" :: "i"(N) : "memory"); }
DI void wg_barrier() {
  asm volatile("s_waitcnt lgkmcnt(0)\n\ts_barrier" ::: "memory");
}
#define SB0() __builtin_amdgcn_sched_barrier(0)
DI float fmax2(float a, float b) { return __builtin_elementwise_maximum(a, b); }
DI unsigned pk2(float a, float b) {
  f32x2 v = {a, b};
  return __builtin_bit_cast(unsigned, __builtin_convertvector(v, bf16x2));
}
DI f32x4 mfma16(bf16x8 a, bf16x8 b, f32x4 c) { return __builtin_amdgcn_mfma_f32_16x16x32_bf16(a, b, c, 0, 0, 0); }
DI unsigned ld_dev(const unsigned* p) { return __hip_atomic_load(p, __ATOMIC_RELAXED, __HIP_MEMORY_SCOPE_AGENT); }
DI void st_dev(unsigned* p, unsigned v) { __hip_atomic_store(p, v, __ATOMIC_RELAXED, __HIP_MEMORY_SCOPE_AGENT); }

__global__ __launch_bounds__(512, 2) void gqa_fwd(const __bf16* __restrict__ Q, const __bf16* __restrict__ K,
                                                  const __bf16* __restrict__ V, __bf16* __restrict__ O, char* WS) {
  __shared__ __attribute__((aligned(16))) char smem[LDS_BYTES];
  LDS_AS char* lds = (LDS_AS char*)smem;
  const unsigned lds0 = (unsigned)(size_t)lds;
  const int tid = threadIdx.x;
  const int lane = tid & 63;
  const int wave = rfl(tid >> 6);
  const int w = wave & 3;
  const int r16 = lane & 15, g = lane >> 4;
  int stn = 0; (void)stn;
  STAMP();
  const int bid = blockIdx.x;
  const int bh = (bid & 7) * 8 + (bid >> 5);
  const int qh = (bid >> 3) & 1, kh = (bid >> 4) & 1;
  const int row0 = (bh * 2 + qh) * SQ;
  const size_t kv0 = ((size_t)bh * SK + kh * (SK / 2)) * DH;

  const __amdgpu_buffer_rsrc_t srdQ = __builtin_amdgcn_make_buffer_rsrc((void*)(Q + (size_t)row0 * DH), (short)0, SQ * ROWB, 0x00020000);
  const __amdgpu_buffer_rsrc_t srdK = __builtin_amdgcn_make_buffer_rsrc((void*)(K + kv0), (short)0, SK / 2 * ROWB, 0x00020000);
  const __amdgpu_buffer_rsrc_t srdV = __builtin_amdgcn_make_buffer_rsrc((void*)(V + kv0), (short)0, SK / 2 * ROWB, 0x00020000);

  unsigned* const fl_own = (unsigned*)(WS + (bid * 4 + w) * 128);
  unsigned* const fl_oth = (unsigned*)(WS + ((bid ^ 16) * 4 + w) * 128);

  const unsigned vsrc_row = (lane >> 3) * ROWB + (((lane & 7) ^ ((lane >> 4) & 3)) << 4);
  if (tid < 2 * SQ) *(LDS_AS float*)(lds + OFF_MX + tid * 4) = -INFINITY;
  if (tid < 2) *(LDS_AS int*)(lds + OFF_GS + tid * 4) = -1;
  LDS_AS int* const gsp = (LDS_AS int*)(lds + OFF_GS);

  asm volatile("s_nop 4" ::: "memory");
#pragma unroll
  for (int i = 0; i < 2; ++i) {
    const int pc = wave * 2 + i, rb = pc >> 2, cb = pc & 3;
    dma16(rfl(lds0 + OFF_K + (rb >> 1) * KSUB + (rb & 1) * 4160 + cb * 1024), vsrc_row, srdK, rfl(rb * 8 * ROWB + cb * 128));
  }
#pragma unroll
  for (int i = 0; i < 9; ++i) {
    const int rb = i * 2 + (wave >> 2), cb = w;
    dma16(rfl(lds0 + (i < 8 ? OFF_Q + i * KSUB : OFF_P) + (rb & 1) * 4160 + cb * 1024), vsrc_row, srdQ, rfl(rb * 8 * ROWB + cb * 128));
  }

  if (wave < 4) {
    const int rh = w & 1, kg = w >> 1;
    wait_vm<0>();
    wg_barrier();
    STAMP();
    const int frag_off = (r16 >> 3) * 4160 + (r16 & 7) * 128 + ((g ^ ((r16 >> 1) & 3)) << 4);
    bf16x8 qf[QRT][8];
    {
      LDS_AS char* qa = lds + OFF_Q + rh * 4 * KSUB + frag_off;
      LDS_AS char* qb = lds + (rh ? OFF_P : OFF_Q + 4 * KSUB) + frag_off;
#pragma unroll
      for (int s = 0; s < 8; ++s)
#pragma unroll
        for (int rt = 0; rt < QRT; ++rt)
          qf[rt][s] = *(LDS_AS bf16x8*)((rt < 4 ? qa + rt * KSUB : qb) + (s >> 1) * 1024 + (s & 1) * 64);
    }
    LDS_AS char* kp = lds + OFF_K + kg * KSUB + frag_off;
    LDS_AS char* pp = lds + OFF_P + rh * 4096 + lane * 16 + kg * 8;
    LDS_AS float* mxp = (LDS_AS float*)(lds + OFF_MX) + rh * 64 + r16;
    float mthr[QRT], mcs[QRT], lsum[QRT];
    f32x4 sacc[2][QRT];
    bf16x8 kf0, kf1;
#pragma unroll
    for (int rt = 0; rt < QRT; ++rt) { mthr[rt] = -INFINITY; mcs[rt] = INFINITY; lsum[rt] = 0.f; }

    auto check = [&](int t, int par, int gs) {
      if (rfl(gs) == t) {
#pragma unroll
        for (int rt = 0; rt < QRT; ++rt) {
          const float mx = mxp[par * SQ + rt * 16];
          const bool gr = mx > mthr[rt];
          const float mcn = gr ? -mx * SC_LOG2E : mcs[rt];
          lsum[rt] *= __builtin_amdgcn_exp2f(mcn - mcs[rt]);
          mcs[rt] = mcn;
          mthr[rt] = gr ? mx + RESCALE_THR : mthr[rt];
        }
      }
    };
    auto sm_rt = [&](int par, int rt) {
      const float mc = mcs[rt];
      float p[4];
#pragma unroll
      for (int i = 0; i < 4; ++i) p[i] = __builtin_amdgcn_exp2f(__builtin_fmaf(sacc[par][rt][i], SC_LOG2E, mc));
      float a = p[0] + p[1], b = p[2] + p[3];
      asm volatile("" : "+v"(a));
      asm volatile("" : "+v"(b));
      a += b;
      asm volatile("" : "+v"(a));
      lsum[rt] += a;
      asm volatile("" : "+v"(lsum[rt]));
      u32x2 pk = {pk2(p[0], p[1]), pk2(p[2], p[3])};
      *(LDS_AS u32x2*)(pp + par * PBUF + rt * 1024) = pk;
    };
    auto kread = [&](LDS_AS char* b, int s) { return *(LDS_AS bf16x8*)(b + (s >> 1) * 1024 + (s & 1) * 64); };
    auto iter = [&](int t, int par, bool sm, bool next) {
      LDS_AS char* kc = kp + (t & 3) * KSLOT;
      LDS_AS char* kn = kp + ((t + 1) & 3) * KSLOT;
      bf16x8 kf[8];
      kf[0] = kf0; kf[1] = kf1;
      int gs = 0;
      if (sm) gs = gsp[par ^ 1];
#pragma unroll
      for (int s = 0; s < 6; ++s) {
        kf[s + 2] = kread(kc, s + 2);
#pragma unroll
        for (int rt = 0; rt < QRT; ++rt) sacc[par][rt] = mfma16(kf[s], qf[rt][s], s == 0 ? f32x4{0.f, 0.f, 0.f, 0.f} : sacc[par][rt]);
        if (sm && s == 0) { SB0(); check(t - 1, par ^ 1, gs); }
        if (!AB_NOSM && sm && s >= 1) {
          sm_rt(par ^ 1, s - 1);
#pragma unroll
          for (int i = 0; i < 5; ++i) {
            __builtin_amdgcn_sched_group_barrier(0x008, 1, 0);
            __builtin_amdgcn_sched_group_barrier(0x002, NVA, 0);
          }
        }
        SB0();
      }
      if (next) { kf0 = kread(kn, 0); kf1 = kread(kn, 1); }
      float m[QRT];
      bool ex = false;
#pragma unroll
      for (int rt = 0; rt <= QRT; ++rt) {
        if (rt < QRT) {
          sacc[par][rt] = mfma16(kf[6], qf[rt][6], sacc[par][rt]);
          sacc[par][rt] = mfma16(kf[7], qf[rt][7], sacc[par][rt]);
        }
        SB0();
        if (rt > 0) {
          const f32x4 x = sacc[par][rt - 1];
          m[rt - 1] = fmax2(fmax2(fmax2(x[0], x[1]), x[2]), x[3]);
          ex |= m[rt - 1] > mthr[rt - 1];
        }
        SB0();
      }
      if (__builtin_amdgcn_ballot_w64(ex)) {
#pragma unroll
        for (int rt = 0; rt < QRT; ++rt)
          __hip_atomic_fetch_max(mxp + par * SQ + rt * 16, m[rt], __ATOMIC_RELAXED, __HIP_MEMORY_SCOPE_WORKGROUP);
        gsp[par] = t;
      }
    };
    kf0 = kread(kp, 0); kf1 = kread(kp, 1);
    wg_barrier();
    STAMP();
    iter(0, 0, false, true);
    STAMP();
    wg_barrier();
#pragma unroll 1
    for (int t = 1; t < NT - 1; t += 2) {
      STAMP();
      iter(t, 1, true, true);
      STAMP();
      wg_barrier();
      STAMP();
      iter(t + 1, 0, true, true);
      STAMP();
      wg_barrier();
    }
    iter(NT - 1, 1, true, false);
    wg_barrier();
    check(NT - 1, 1, gsp[1]);
#pragma unroll
    for (int rt = 0; rt < QRT; ++rt) sm_rt(1, rt);
#pragma unroll
    for (int rt = 0; rt < QRT; ++rt) {
      const unsigned u0 = __builtin_bit_cast(unsigned, lsum[rt]);
      const auto x = __builtin_amdgcn_permlane16_swap(u0, u0, false, false);
      const unsigned x0 = x[0], x1 = x[1];
      const unsigned u1 = __builtin_bit_cast(unsigned, __builtin_bit_cast(float, x0) + __builtin_bit_cast(float, x1));
      const auto y = __builtin_amdgcn_permlane32_swap(u1, u1, false, false);
      const unsigned y0 = y[0], y1 = y[1];
      *(LDS_AS float*)(lds + OFF_L + (((rh * 4 + rt) * 16 + r16) * 2 + kg) * 4) = __builtin_bit_cast(float, y0) + __builtin_bit_cast(float, y1);
    }
    wg_barrier();
    STAMP();
    STAMP_DUMP();
  } else {
    const unsigned vsrc_v = (16 * (lane >> 5) + 8 * ((lane >> 4) & 1) + ((lane >> 3) & 1)) * ROWB + (lane & 7) * 16 + w * 128;
    const unsigned kdst = lds0 + OFF_K + (w >> 1) * KSUB + (w & 1) * 4160;
    const unsigned vdst = lds0 + OFF_V + w * VSUB;
    auto dma_k = [&](int t, int cb) { dma16(kdst + (t & 3) * KSLOT + cb * 1024, vsrc_row, srdK, (t * KT + w * 8) * ROWB + cb * 128); };
    auto dma_v = [&](int t, int pc) { dma16(vdst + ((t + 3) & 3) * VSLOT + pc * 1056, vsrc_v, srdV, (t * KT + pc * 2) * ROWB); };
    unsigned nonce;
    asm volatile("global_load_dword %0, %1, off offset:64 sc1" : "=v"(nonce) : "v"(fl_own) : "memory");
#pragma unroll
    for (int pc = 0; pc < 4; ++pc) dma_k(1, pc);
#pragma unroll
    for (int pc = 0; pc < 4; ++pc) dma_v(0, pc);
#pragma unroll
    for (int pc = 0; pc < 4; ++pc) dma_k(2, pc);
    asm volatile("s_waitcnt vmcnt(12)" : "+v"(nonce) :: "memory");
    wg_barrier();
    STAMP();
    nonce += 1;
    asm volatile("global_store_dword %0, %1, off sc1" :: "v"(fl_own), "v"(nonce) : "memory");
    const int kk = 4 * g + (r16 >> 2), pq = lane & 3;
    LDS_AS char* vp = lds + OFF_V + w * VSUB + ((kk >> 1) & 3) * 1056 + (kk & 1) * 128 + (kk >> 3) * 256 + pq * 8;
    LDS_AS char* vpa = vp + kh * 64;
    LDS_AS char* vpb = vp + (kh ^ 1) * 64;
    LDS_AS char* pp = lds + OFF_P + lane * 16;
    LDS_AS float* mxp = (LDS_AS float*)(lds + OFF_MX) + r16;
    f32x4 oacc[4][NRT];
    float mthr[NRT], mcs[NRT];
#pragma unroll
    for (int rt = 0; rt < NRT; ++rt) {
      mthr[rt] = -INFINITY; mcs[rt] = INFINITY;
#pragma unroll
      for (int c = 0; c < 4; ++c) oacc[c][rt] = f32x4{0.f, 0.f, 0.f, 0.f};
    }
    bf16x8 vf[4];
    auto load_v = [&](int t, int c) {
      LDS_AS char* b = (c < 2 ? vpa : vpb) + ((t + 3) & 3) * VSLOT + (c & 1) * 32;
      const s16x4 x = __builtin_amdgcn_ds_read_tr16_b64_v4i16((LDS_AS s16x4*)b);
      const s16x4 y = __builtin_amdgcn_ds_read_tr16_b64_v4i16((LDS_AS s16x4*)(b + 512));
      vf[c] = __builtin_bit_cast(bf16x8, __builtin_shufflevector(x, y, 0, 1, 2, 3, 4, 5, 6, 7));
    };
    auto pv = [&](int i, bool mm, bool upd) {
      LDS_AS char* pc = pp + (i & 1) * PBUF;
      LDS_AS float* mc = mxp + ((i & 1) ^ 1) * SQ;
      bf16x8 pf[NRT];
      int gs = 0;
      if (upd) gs = gsp[(i & 1) ^ 1];
      if (mm) {
#pragma unroll
        for (int rt = 0; rt < NRT; ++rt) pf[rt] = *(LDS_AS bf16x8*)(pc + rt * 1024);
      }
      SB0();
#pragma unroll
      for (int cc = 0; cc < 4; ++cc) {
        const int c = cc ^ 2;
#pragma unroll
        for (int rt = 0; rt < NRT; ++rt) {
          if (mm && !AB_NOPV) oacc[c][rt] = mfma16(vf[c], pf[rt], oacc[c][rt]);
          if (rt == 3 || rt == 7) {
            const int d = cc * 2 + (rt >> 2);
            SB0();
            if (AB_NODMA && i > 1) {} else
            if (d < 4) { if (i + 1 < NT) dma_v(i + 1, d); }
            else { if (i + 3 < NT) dma_k(i + 3, d - 4); }
            SB0();
          }
        }
        if (upd) load_v(i - 1, c);
        SB0();
      }
      if (upd && rfl(gs) == i - 1) {
#pragma unroll
        for (int rt = 0; rt < NRT; ++rt) {
          const float mx = mc[rt * 16];
          const bool gr = mx > mthr[rt];
          const float mcn = gr ? -mx * SC_LOG2E : mcs[rt];
          const float al = __builtin_amdgcn_exp2f(mcn - mcs[rt]);
          mcs[rt] = mcn;
          mthr[rt] = gr ? mx + RESCALE_THR : mthr[rt];
#pragma unroll
          for (int c = 0; c < 4; ++c) oacc[c][rt] *= al;
        }
      }
    };
    wait_vm<8>();
    wg_barrier();
    pv(0, false, false);
    wait_vm<8>();
    wg_barrier();
    pv(1, false, true);
    wait_vm<8>();
    wg_barrier();
    if (PRIO_PV) __builtin_amdgcn_s_setprio(PRIO_PV);
#pragma unroll 1
    for (int i = 2;; ++i) {
      STAMP();
      pv(i, true, true);
      if (i == NT + 1) break;
      if (i < NT - 3) wait_vm<8>(); else if (i < NT - 1) wait_vm<4>(); else wait_vm<0>();
      STAMP();
      wg_barrier();
    }
    unsigned echoed = ld_dev(fl_oth);
    float lse[12], inv[NRT];
    u32x4 tx[NRT];
#pragma unroll
    for (int rt = 0; rt < NRT; ++rt) {
      const f32x2 s2 = *(const LDS_AS f32x2*)(lds + OFF_L + (rt * 16 + r16) * 8);
      const float l = s2[0] + s2[1];
      inv[rt] = __builtin_amdgcn_rcpf(l);
      lse[rt] = __builtin_amdgcn_logf(l) - mcs[rt];
    }
    lse[9] = lse[10] = lse[11] = 0.f;
    STAMP();
    const __amdgpu_buffer_rsrc_t srdS = __builtin_amdgcn_make_buffer_rsrc(WS + WS_PAY + (size_t)(bid * 4 + w) * PAYB, (short)0, PAYB, 0x00020000);
    const __amdgpu_buffer_rsrc_t srdR = __builtin_amdgcn_make_buffer_rsrc(WS + WS_PAY + (size_t)((bid ^ 16) * 4 + w) * PAYB, (short)0, PAYB, 0x00020000);
#pragma unroll
    for (int rt = 0; rt < NRT; ++rt) {
      const f32x4 a = oacc[2][rt] * inv[rt], b = oacc[3][rt] * inv[rt];
      tx[rt] = u32x4{pk2(a[0], a[1]), pk2(a[2], a[3]), pk2(b[0], b[1]), pk2(b[2], b[3])};
    }
    SB0();
#pragma unroll
    for (int rt = 0; rt < NRT; ++rt) __builtin_amdgcn_raw_buffer_store_b128(tx[rt], srdS, lane * 16, rt * 1024, 16);
    asm volatile("s_nop 1" ::: "memory");
    SB0();
    if (g == 0) {
#pragma unroll
      for (int u = 0; u < 3; ++u) {
        const f32x4 x = {lse[4 * u], lse[4 * u + 1], lse[4 * u + 2], lse[4 * u + 3]};
        __builtin_amdgcn_raw_buffer_store_b128(__builtin_bit_cast(u32x4, x), srdS, r16 * 16, 9 * 1024 + u * 256, 16);
      }
    }
    SB0();
    wait_vm<0>();
    STAMP();
    st_dev(fl_oth + 16, echoed);
    while (true) {
      const unsigned a = ld_dev(fl_own + 16);
      const unsigned r = ld_dev(fl_oth);
      if (rfl(r) != rfl(echoed)) { echoed = r; st_dev(fl_oth + 16, r); }
      if (rfl(a) == rfl(nonce)) break;
    }
    asm volatile("s_waitcnt vmcnt(0)" ::: "memory");
    STAMP();
    const unsigned rlast = ld_dev(fl_oth);
    f32x4 ml[3];
    u32x4 rx[NRT];
#pragma unroll
    for (int u = 0; u < 3; ++u) ml[u] = __builtin_bit_cast(f32x4, __builtin_amdgcn_raw_buffer_load_b128(srdR, r16 * 16, 9 * 1024 + u * 256, 16));
#pragma unroll
    for (int rt = 0; rt < NRT; ++rt) rx[rt] = __builtin_amdgcn_raw_buffer_load_b128(srdR, lane * 16, rt * 1024, 16);
    if (rfl(rlast) != rfl(echoed)) st_dev(fl_oth + 16, rlast);
#pragma unroll
    for (int rt = 0; rt < NRT; ++rt) {
      const float lo = ml[rt >> 2][rt & 3];
      const float mm = fmaxf(lse[rt], lo);
      const float ea = __builtin_amdgcn_exp2f(lse[rt] - mm), eb = __builtin_amdgcn_exp2f(lo - mm);
      const float rs = __builtin_amdgcn_rcpf(ea + eb);
      const float fa = ea * rs * inv[rt], fb = eb * rs;
      f32x4 pa, pb;
#pragma unroll
      for (int j = 0; j < 2; ++j) {
        pa[2 * j] = __builtin_bit_cast(float, rx[rt][j] << 16); pa[2 * j + 1] = __builtin_bit_cast(float, rx[rt][j] & 0xffff0000u);
        pb[2 * j] = __builtin_bit_cast(float, rx[rt][2 + j] << 16); pb[2 * j + 1] = __builtin_bit_cast(float, rx[rt][2 + j] & 0xffff0000u);
      }
      const f32x4 a = oacc[0][rt] * fa + pa * fb, b = oacc[1][rt] * fa + pb * fb;
      const auto x = __builtin_amdgcn_permlane16_swap(pk2(a[0], a[1]), pk2(b[0], b[1]), false, false);
      const auto y = __builtin_amdgcn_permlane16_swap(pk2(a[2], a[3]), pk2(b[2], b[3]), false, false);
      const u32x4 o = {x[0], y[0], x[1], y[1]};
      *(u32x4*)(O + (size_t)(row0 + rt * 16 + r16) * DH + w * 64 + kh * 32 + (g & 1) * 16 + (g >> 1) * 8) = o;
    }
    STAMP();
    STAMP_DUMP();
  }
}

extern "C" void kernel_launch(void* const* d_in, const int* in_sizes, int n_in, void* d_out, int out_size,
                              void* d_ws, size_t ws_size, hipStream_t stream) {
  (void)in_sizes; (void)n_in; (void)out_size; (void)ws_size;
  gqa_fwd<<<NB * HQ * 2, 512, 0, stream>>>((const __bf16*)d_in[0], (const __bf16*)d_in[1], (const __bf16*)d_in[2], (__bf16*)d_out, (char*)d_ws);
}
```

```cpp
#include <hip/hip_runtime.h>
#include <hip/hip_bf16.h>

typedef __attribute__((ext_vector_type(8))) __bf16 bf16x8;
typedef __attribute__((ext_vector_type(2))) __bf16 bf16x2;
typedef __attribute__((ext_vector_type(4))) float f32x4;
typedef __attribute__((ext_vector_type(2))) float f32x2;
typedef __attribute__((ext_vector_type(4))) short s16x4;
typedef __attribute__((ext_vector_type(8))) short s16x8;
typedef __attribute__((ext_vector_type(4))) unsigned u32x4;
typedef __attribute__((ext_vector_type(2))) unsigned u32x2;
#define LDS_AS __attribute__((address_space(3)))
#define DI __device__ __forceinline__

constexpr int NB = 8, HQ = 16, HKV = 8, SQ = 144, SK = 1024, DH = 256;
constexpr int ROWS_KV = (HQ / HKV) * SQ;
constexpr int ROWS_WG = ROWS_KV / 4;
constexpr int NRT = 5;
constexpr int KT = 64;
constexpr int NT = SK / KT;
constexpr int ROWB = DH * 2;

constexpr int KSLOT = 2 * 4096 + 64;
constexpr int VSLOT = 8 * 1056;
constexpr int PBUF = NRT * 2 * 1024;
constexpr int OFF_K = 0;
constexpr int OFF_V0 = OFF_K + 8 * KSLOT;
constexpr int OFF_V1 = OFF_V0 + 4 * VSLOT;
constexpr int OFF_P = OFF_V1 + 4 * VSLOT;
constexpr int OFF_MX = OFF_P + 2 * PBUF;
constexpr int OFF_L = OFF_MX + 2 * 80 * 4;
constexpr int OFF_PF = OFF_L + 80 * 16 * 4;
constexpr int LDS_BYTES = OFF_PF + 256;
#define STAMP() do {} while (0)
#define STAMP_DUMP() do {} while (0)
constexpr int OFF_Q = OFF_V1;
static_assert(OFF_Q + NRT * KSLOT <= OFF_MX, "Q staging too large");
static_assert(LDS_BYTES <= 163840, "LDS");

constexpr float SC_LOG2E = 0.0625f * 1.4426950408889634f;
constexpr float RESCALE_THR = 8.0f / SC_LOG2E;

#define PF_DIST 0
#define POL "nt"
#ifndef NVA
#define NVA 3
#endif
#define PRIO_QK 0
#define PRIO_PV 0

DI unsigned rfl(unsigned x) { return __builtin_amdgcn_readfirstlane(x); }
DI void dma16(unsigned m0v, unsigned voff, __amdgpu_buffer_rsrc_t srd, unsigned soff) {
  asm volatile("s_mov_b32 m0, %0\n\ts_nop 0\n\tbuffer_load_dwordx4 %1, %2, %3 offen " POL " lds"
               :: "s"(m0v), "v"(voff), "s"(srd), "s"(soff) : "memory");
}
DI void dma4(unsigned m0v, unsigned voff, __amdgpu_buffer_rsrc_t srd, unsigned soff) {
  asm volatile("s_mov_b32 m0, %0\n\ts_nop 0\n\tbuffer_load_dword %1, %2, %3 offen lds"
               :: "s"(m0v), "v"(voff), "s"(srd), "s"(soff) : "memory");
}
template <int N> DI void wait_vm() { asm volatile("s_waitcnt vmcnt(%0)" :: "i"(N) : "memory"); }
DI void wg_barrier() {
  asm volatile("s_waitcnt lgkmcnt(0)\n\ts_barrier" ::: "memory");
}
#define SB0() __builtin_amdgcn_sched_barrier(0)
DI float fmax2(float a, float b) { return __builtin_elementwise_maximum(a, b); }
DI unsigned pk2(float a, float b) {
  f32x2 v = {a, b};
  return __builtin_bit_cast(unsigned, __builtin_convertvector(v, bf16x2));
}
DI f32x4 mfma16(bf16x8 a, bf16x8 b, f32x4 c) { return __builtin_amdgcn_mfma_f32_16x16x32_bf16(a, b, c, 0, 0, 0); }

__global__ __launch_bounds__(512, 2) void gqa_fwd(const __bf16* __restrict__ Q, const __bf16* __restrict__ K,
                                                  const __bf16* __restrict__ V, __bf16* __restrict__ O) {
  __shared__ __attribute__((aligned(16))) char smem[LDS_BYTES];
  LDS_AS char* lds = (LDS_AS char*)smem;
  const unsigned lds0 = (unsigned)(size_t)lds;
  const int tid = threadIdx.x;
  const int lane = tid & 63;
  const int wave = rfl(tid >> 6);
  const int w = wave & 3;
  const int r16 = lane & 15, g = lane >> 4;
  int stn = 0; (void)stn;
  STAMP();
  const int bid = blockIdx.x;
  const int bh = (bid & 7) * 8 + (bid >> 5);
  const int jq = (bid >> 3) & 3;
  const int row0 = bh * ROWS_KV + jq * ROWS_WG;

  const __amdgpu_buffer_rsrc_t srdQ = __builtin_amdgcn_make_buffer_rsrc((void*)(Q + (size_t)row0 * DH), (short)0, ROWS_WG * ROWB, 0x00020000);
  const __amdgpu_buffer_rsrc_t srdK = __builtin_amdgcn_make_buffer_rsrc((void*)(K + (size_t)bh * SK * DH), (short)0, SK * ROWB, 0x00020000);
  const __amdgpu_buffer_rsrc_t srdV = __builtin_amdgcn_make_buffer_rsrc((void*)(V + (size_t)bh * SK * DH), (short)0, SK * ROWB, 0x00020000);

  const unsigned vsrc_row = (lane >> 3) * ROWB + (((lane & 7) ^ ((lane >> 4) & 3)) << 4);
  if (tid < 160) *(LDS_AS float*)(lds + OFF_MX + tid * 4) = -INFINITY;

  asm volatile("s_nop 4" ::: "memory");
#pragma unroll
  for (int i = 0; i < 5; ++i) {
    const int pc = wave * 5 + i;
    const int rb = pc >> 2, cb = pc & 3;
    const int rbs = rb > 8 ? 8 : rb;
    dma16(rfl(lds0 + OFF_Q + (rb >> 1) * KSLOT + (rb & 1) * 4160 + cb * 1024), vsrc_row, srdQ, rfl(rbs * 8 * ROWB + cb * 128));
  }

  if (wave < 4) {
    const unsigned kbase = lds0 + OFF_K + w * 2 * KSLOT;
    auto dma_k = [&](int t, int pc) {
      const int rb = pc & 1, cb = pc >> 1;
      dma16(kbase + (t & 1) * KSLOT + rb * 4160 + cb * 1024, vsrc_row, srdK, (t * KT + w * 16 + rb * 8) * ROWB + cb * 128);
    };
#pragma unroll
    for (int pc = 0; pc < 8; ++pc) dma_k(0, pc);
#pragma unroll
    for (int pc = 0; pc < 8; ++pc) dma_k(1, pc);
    wait_vm<16>();
    wg_barrier();
    STAMP();
    const int frag_off = (r16 >> 3) * 4160 + (r16 & 7) * 128 + ((g ^ ((r16 >> 1) & 3)) << 4);
    bf16x8 qf[NRT][8];
#pragma unroll
    for (int rt = 0; rt < NRT; ++rt)
#pragma unroll
      for (int s = 0; s < 8; ++s)
        qf[rt][s] = *(LDS_AS bf16x8*)(lds + OFF_Q + rt * KSLOT + frag_off + (s >> 1) * 1024 + (s & 1) * 64);
    LDS_AS char* kp = lds + OFF_K + w * 2 * KSLOT + frag_off;
    LDS_AS char* pp = lds + OFF_P + (w >> 1) * 1024 + lane * 16 + (w & 1) * 8;
    LDS_AS float* mxp = (LDS_AS float*)(lds + OFF_MX) + r16;
    float mthr[NRT], mcs[NRT], lsum[NRT], mx[NRT];
    f32x4 sacc[2][NRT];
    bf16x8 kf0, kf1;
#pragma unroll
    for (int rt = 0; rt < NRT; ++rt) { mthr[rt] = -INFINITY; mcs[rt] = INFINITY; lsum[rt] = 0.f; }
    if (PRIO_QK) __builtin_amdgcn_s_setprio(PRIO_QK);

    auto mxread = [&](int par) {
#pragma unroll
      for (int rt = 0; rt < NRT; ++rt) mx[rt] = mxp[par * 80 + rt * 16];
    };
    auto check = [&]() {
      bool grow = false;
#pragma unroll
      for (int rt = 0; rt < NRT; ++rt) grow |= mx[rt] > mthr[rt];
      if (__builtin_amdgcn_ballot_w64(grow)) {
#pragma unroll
        for (int rt = 0; rt < NRT; ++rt) {
          const bool gr = mx[rt] > mthr[rt];
          const float mcn = gr ? -mx[rt] * SC_LOG2E : mcs[rt];
          lsum[rt] *= __builtin_amdgcn_exp2f(mcn - mcs[rt]);
          mcs[rt] = mcn;
          mthr[rt] = gr ? mx[rt] + RESCALE_THR : mthr[rt];
        }
      }
    };
    auto sm_rt = [&](int par, int rt) {
      const float mc = mcs[rt];
      float p[4];
#pragma unroll
      for (int i = 0; i < 4; ++i) p[i] = __builtin_amdgcn_exp2f(__builtin_fmaf(sacc[par][rt][i], SC_LOG2E, mc));
      float a = p[0] + p[1], b = p[2] + p[3];
      asm volatile("" : "+v"(a));
      lsum[rt] += a + b;
      u32x2 pk = {pk2(p[0], p[1]), pk2(p[2], p[3])};
      *(LDS_AS u32x2*)(pp + par * PBUF + rt * 2048) = pk;
    };
    auto kread = [&](int par, int s) { return *(LDS_AS bf16x8*)(kp + par * KSLOT + (s >> 1) * 1024 + (s & 1) * 64); };
    auto iter = [&](int t, int par, bool sm, bool more, int nextwait) {
      bf16x8 kf[8];
      kf[0] = kf0; kf[1] = kf1;
      if (sm) mxread(par ^ 1);
#pragma unroll
      for (int s = 0; s < 8; ++s) {
        if (s + 2 < 8) kf[s + 2] = kread(par, s + 2);
#pragma unroll
        for (int rt = 0; rt < NRT; ++rt) sacc[par][rt] = mfma16(kf[s], qf[rt][s], s == 0 ? f32x4{0.f, 0.f, 0.f, 0.f} : sacc[par][rt]);
        if (sm && s == 1) { SB0(); check(); }
        if (sm && s >= 2 && s <= 6) {
          sm_rt(par ^ 1, s - 2);
#pragma unroll
          for (int i = 0; i < 5; ++i) {
            __builtin_amdgcn_sched_group_barrier(0x008, 1, 0);
            __builtin_amdgcn_sched_group_barrier(0x002, NVA, 0);
          }
        }
        SB0();
        if (more && s >= 1) dma_k(t + 2, s - 1);
        SB0();
      }
      if (more) dma_k(t + 2, 7);
      if (nextwait == 8) wait_vm<8>(); else if (nextwait == 0) wait_vm<0>();
      if (nextwait >= 0) { kf0 = kread(par ^ 1, 0); kf1 = kread(par ^ 1, 1); }
#pragma unroll
      for (int rt = 0; rt < NRT; ++rt) {
        const float m = fmax2(fmax2(sacc[par][rt][0], sacc[par][rt][1]), fmax2(sacc[par][rt][2], sacc[par][rt][3]));
        __hip_atomic_fetch_max(mxp + par * 80 + rt * 16, m, __ATOMIC_RELAXED, __HIP_MEMORY_SCOPE_WORKGROUP);
      }
    };
    STAMP();
    wait_vm<8>();
    kf0 = kread(0, 0); kf1 = kread(0, 1);
    STAMP();
    iter(0, 0, false, true, 8);
    STAMP();
    wg_barrier();
#pragma unroll 1
    for (int t = 1; t < NT - 3; t += 2) {
      STAMP();
      iter(t, 1, true, true, 8);
      STAMP();
      wg_barrier();
      STAMP();
      iter(t + 1, 0, true, true, 8);
      STAMP();
      wg_barrier();
    }
    iter(13, 1, true, true, 8);   wg_barrier();
    iter(14, 0, true, false, 0);  wg_barrier();
    iter(15, 1, true, false, -1); wg_barrier();
    mxread(1);
    check();
#pragma unroll
    for (int rt = 0; rt < NRT; ++rt) sm_rt(1, rt);
#pragma unroll
    for (int rt = 0; rt < NRT; ++rt) *(LDS_AS float*)(lds + OFF_L + ((rt * 16 + r16) * 16 + w * 4 + g) * 4) = lsum[rt];
    wg_barrier();
    STAMP();
    STAMP_DUMP();
  } else {
    const unsigned vsrc_v = (16 * (lane >> 4) + ((lane >> 3) & 1)) * ROWB + (lane & 7) * 16 + w * 128;
    const unsigned vbase = lds0 + OFF_V0 + w * VSLOT;
    auto dma_v = [&](int t, int pc) {
      dma16(vbase + (t & 1) * (4 * VSLOT) + pc * 1056, vsrc_v, srdV, t * KT * ROWB + pc * 1024);
    };
    const unsigned vsrc_pf = (jq * 64 + lane) * 128 + (w & 1) * 64;
    const __amdgpu_buffer_rsrc_t srdPF = __builtin_amdgcn_make_buffer_rsrc((void*)((w < 2 ? K : V) + (size_t)bh * SK * DH), (short)0, SK * ROWB, 0x00020000);
    const int pf_add = w < 2 ? 2 : 0;
    auto prefetch = [&](int n) {
    };
#pragma unroll
    for (int pc = 0; pc < 8; ++pc) dma_v(0, pc);
    wait_vm<8>();
    wg_barrier();
    STAMP();
    const int kk = 4 * g + (r16 >> 2), pq = lane & 3;
    LDS_AS char* vp = lds + OFF_V0 + w * VSLOT + (kk >> 1) * 1056 + (kk & 1) * 128 + pq * 8;
    LDS_AS char* pp = lds + OFF_P + lane * 16;
    LDS_AS float* mxp = (LDS_AS float*)(lds + OFF_MX) + r16;
    if (PRIO_PV) __builtin_amdgcn_s_setprio(PRIO_PV);
    f32x4 oacc[4][NRT];
    float mthr[NRT], mcs[NRT];
#pragma unroll
    for (int rt = 0; rt < NRT; ++rt) {
      mthr[rt] = -INFINITY; mcs[rt] = INFINITY;
#pragma unroll
      for (int c = 0; c < 4; ++c) oacc[c][rt] = f32x4{0.f, 0.f, 0.f, 0.f};
    }
    bf16x8 vf[2][4];
    auto load_v = [&](int par) {
#pragma unroll
      for (int ks = 0; ks < 2; ++ks)
#pragma unroll
        for (int c = 0; c < 4; ++c) {
          const s16x4 a = __builtin_amdgcn_ds_read_tr16_b64_v4i16((LDS_AS s16x4*)(vp + par * (4 * VSLOT) + (2 * ks) * 256 + c * 32));
          const s16x4 b = __builtin_amdgcn_ds_read_tr16_b64_v4i16((LDS_AS s16x4*)(vp + par * (4 * VSLOT) + (2 * ks + 1) * 256 + c * 32));
          vf[ks][c] = __builtin_bit_cast(bf16x8, __builtin_shufflevector(a, b, 0, 1, 2, 3, 4, 5, 6, 7));
        }
    };
    auto rescale = [&](int par) {
      float mx[NRT];
#pragma unroll
      for (int rt = 0; rt < NRT; ++rt) mx[rt] = mxp[par * 80 + rt * 16];
      bool grow = false;
#pragma unroll
      for (int rt = 0; rt < NRT; ++rt) grow |= mx[rt] > mthr[rt];
      if (__builtin_amdgcn_ballot_w64(grow)) {
#pragma unroll
        for (int rt = 0; rt < NRT; ++rt) {
          const bool gr = mx[rt] > mthr[rt];
          const float mcn = gr ? -mx[rt] * SC_LOG2E : mcs[rt];
          const float al = __builtin_amdgcn_exp2f(mcn - mcs[rt]);
          mcs[rt] = mcn;
          mthr[rt] = gr ? mx[rt] + RESCALE_THR : mthr[rt];
#pragma unroll
          for (int c = 0; c < 4; ++c) oacc[c][rt] *= al;
        }
      }
    };
    auto pv = [&](int tn, int par, bool more) {
      bf16x8 pf[2][NRT];
#pragma unroll
      for (int ks = 0; ks < 2; ++ks)
#pragma unroll
        for (int rt = 0; rt < NRT; ++rt) pf[ks][rt] = *(LDS_AS bf16x8*)(pp + par * PBUF + rt * 2048 + ks * 1024);
      SB0();
#pragma unroll
      for (int ks = 0; ks < 2; ++ks)
#pragma unroll
        for (int rt = 0; rt < NRT; ++rt) {
#pragma unroll
          for (int c = 0; c < 4; ++c) oacc[c][rt] = mfma16(vf[ks][c], pf[ks][rt], oacc[c][rt]);
          SB0();
          const int i = ks * NRT + rt;
          if (more && i < 8) dma_v(tn, i);
          SB0();
        }
    };
    constexpr int PFN = 0, PF0 = 0;
    wg_barrier();
#pragma unroll
    for (int pc = 0; pc < 8; ++pc) dma_v(1, pc);
    rescale(0);
    wait_vm<8 + PF0>();
    load_v(0);
    prefetch(1 + PF_DIST);
    wg_barrier();
#pragma unroll 1
    for (int t = 2; t < NT; t += 2) {
      STAMP();
      pv(t, 0, true);
      rescale(1);
      wait_vm<8 + PFN>();
      load_v(1);
      prefetch(t + PF_DIST);
      STAMP();
      wg_barrier();
      STAMP();
      pv(t + 1, 1, true);
      rescale(0);
      wait_vm<8 + PFN>();
      load_v(0);
      prefetch(t + 1 + PF_DIST);
      STAMP();
      wg_barrier();
    }
    pv(0, 0, false);
    rescale(1);
    wait_vm<0>();
    load_v(1);
    wg_barrier();
    pv(0, 1, false);
    STAMP();
#pragma unroll
    for (int rt = 0; rt < NRT; ++rt) {
      const LDS_AS f32x4* lp = (const LDS_AS f32x4*)(lds + OFF_L + (rt * 16 + r16) * 64);
      const f32x4 s4 = (lp[0] + lp[1]) + (lp[2] + lp[3]);
      const float inv = 1.0f / ((s4[0] + s4[1]) + (s4[2] + s4[3]));
      const int row = rt * 16 + r16;
#pragma unroll
      for (int u = 0; u < 2; ++u) {
        const f32x4 a = oacc[2 * u][rt] * inv, b = oacc[2 * u + 1][rt] * inv;
        const auto x = __builtin_amdgcn_permlane16_swap(pk2(a[0], a[1]), pk2(b[0], b[1]), false, false);
        const auto y = __builtin_amdgcn_permlane16_swap(pk2(a[2], a[3]), pk2(b[2], b[3]), false, false);
        const u32x4 o = {x[0], y[0], x[1], y[1]};
        if (row < ROWS_WG)
          __builtin_nontemporal_store(o, (u32x4*)(O + (size_t)(row0 + row) * DH + w * 64 + u * 32 + (g & 1) * 16 + (g >> 1) * 8));
      }
    }
    STAMP();
    STAMP_DUMP();
  }
}

extern "C" void kernel_launch(void* const* d_in, const int* in_sizes, int n_in, void* d_out, int out_size,
                              void* d_ws, size_t ws_size, hipStream_t stream) {
  (void)in_sizes; (void)n_in; (void)out_size; (void)d_ws; (void)ws_size;
  gqa_fwd<<<NB * HKV * 4, 512, 0, stream>>>((const __bf16*)d_in[0], (const __bf16*)d_in[1], (const __bf16*)d_in[2], (__bf16*)d_out);
}
```

```cpp
#include <hip/hip_runtime.h>
#include <hip/hip_bf16.h>

typedef __attribute__((ext_vector_type(8))) __bf16 bf16x8;
typedef __attribute__((ext_vector_type(2))) __bf16 bf16x2;
typedef __attribute__((ext_vector_type(4))) float f32x4;
typedef __attribute__((ext_vector_type(2))) float f32x2;
typedef __attribute__((ext_vector_type(4))) short s16x4;
typedef __attribute__((ext_vector_type(8))) short s16x8;
typedef __attribute__((ext_vector_type(4))) unsigned u32x4;
typedef __attribute__((ext_vector_type(2))) unsigned u32x2;
#define LDS_AS __attribute__((address_space(3)))
#define DI __device__ __forceinline__

constexpr int NB = 8, HQ = 16, HKV = 8, SQ = 144, SK = 1024, DH = 256;
constexpr int ROWS_KV = (HQ / HKV) * SQ;
constexpr int ROWS_WG = ROWS_KV / 4;
constexpr int NRT = 5;
constexpr int KT = 64;
constexpr int NT = SK / KT;
constexpr int ROWB = DH * 2;

constexpr int KSLOT = 2 * 4096 + 64;
constexpr int VSLOT = 8 * 1056;
constexpr int PBUF = NRT * 2 * 1024;
constexpr int OFF_K = 0;
constexpr int OFF_V0 = OFF_K + 8 * KSLOT;
constexpr int OFF_V1 = OFF_V0 + 4 * VSLOT;
constexpr int OFF_P = OFF_V1 + 4 * VSLOT;
constexpr int OFF_MX = OFF_P + 2 * PBUF;
constexpr int OFF_L = OFF_MX + 2 * 80 * 4;
constexpr int OFF_PF = OFF_L + 80 * 16 * 4;
constexpr int LDS_BYTES = OFF_PF + 256;
#define STAMP() do {} while (0)
#define STAMP_DUMP() do {} while (0)
constexpr int OFF_Q = OFF_V1;
static_assert(OFF_Q + NRT * KSLOT <= OFF_MX, "Q staging too large");
static_assert(LDS_BYTES <= 163840, "LDS");

constexpr float SC_LOG2E = 0.0625f * 1.4426950408889634f;
constexpr float RESCALE_THR = 8.0f / SC_LOG2E;

#define PF_DIST 0
#define POL "nt"
#ifndef NVA
#define NVA 3
#endif
#define PRIO_QK 0
#define PRIO_PV 0

DI unsigned rfl(unsigned x) { return __builtin_amdgcn_readfirstlane(x); }
DI void dma16(unsigned m0v, unsigned voff, __amdgpu_buffer_rsrc_t srd, unsigned soff) {
  asm volatile("s_mov_b32 m0, %0\n\ts_nop 0\n\tbuffer_load_dwordx4 %1, %2, %3 offen " POL " lds"
               :: "s"(m0v), "v"(voff), "s"(srd), "s"(soff) : "memory");
}
DI void dma4(unsigned m0v, unsigned voff, __amdgpu_buffer_rsrc_t srd, unsigned soff) {
  asm volatile("s_mov_b32 m0, %0\n\ts_nop 0\n\tbuffer_load_dword %1, %2, %3 offen lds"
               :: "s"(m0v), "v"(voff), "s"(srd), "s"(soff) : "memory");
}
template <int N> DI void wait_vm() { asm volatile("s_waitcnt vmcnt(%0)" :: "i"(N) : "memory"); }
DI void wg_barrier() {
  asm volatile("s_waitcnt lgkmcnt(0)\n\ts_barrier" ::: "memory");
}
#define SB0() __builtin_amdgcn_sched_barrier(0)
DI float fmax2(float a, float b) { return __builtin_elementwise_maximum(a, b); }
DI unsigned pk2(float a, float b) {
  f32x2 v = {a, b};
  return __builtin_bit_cast(unsigned, __builtin_convertvector(v, bf16x2));
}
DI f32x4 mfma16(bf16x8 a, bf16x8 b, f32x4 c) { return __builtin_amdgcn_mfma_f32_16x16x32_bf16(a, b, c, 0, 0, 0); }

__global__ __launch_bounds__(512, 2) void gqa_fwd(const __bf16* __restrict__ Q, const __bf16* __restrict__ K,
                                                  const __bf16* __restrict__ V, __bf16* __restrict__ O) {
  __shared__ __attribute__((aligned(16))) char smem[LDS_BYTES];
  LDS_AS char* lds = (LDS_AS char*)smem;
  const unsigned lds0 = (unsigned)(size_t)lds;
  const int tid = threadIdx.x;
  const int lane = tid & 63;
  const int wave = rfl(tid >> 6);
  const int w = wave & 3;
  const int r16 = lane & 15, g = lane >> 4;
  int stn = 0; (void)stn;
  STAMP();
  const int bid = blockIdx.x;
  const int bh = (bid & 7) * 8 + (bid >> 5);
  const int jq = (bid >> 3) & 3;
  const int row0 = bh * ROWS_KV + jq * ROWS_WG;

  const __amdgpu_buffer_rsrc_t srdQ = __builtin_amdgcn_make_buffer_rsrc((void*)(Q + (size_t)row0 * DH), (short)0, ROWS_WG * ROWB, 0x00020000);
  const __amdgpu_buffer_rsrc_t srdK = __builtin_amdgcn_make_buffer_rsrc((void*)(K + (size_t)bh * SK * DH), (short)0, SK * ROWB, 0x00020000);
  const __amdgpu_buffer_rsrc_t srdV = __builtin_amdgcn_make_buffer_rsrc((void*)(V + (size_t)bh * SK * DH), (short)0, SK * ROWB, 0x00020000);

  const unsigned vsrc_row = (lane >> 3) * ROWB + (((lane & 7) ^ ((lane >> 4) & 3)) << 4);
  if (tid < 160) *(LDS_AS float*)(lds + OFF_MX + tid * 4) = -INFINITY;
  LDS_AS int* const gsp = (LDS_AS int*)(lds + OFF_PF);
  if (tid < 2) gsp[tid] = -1;

  asm volatile("s_nop 4" ::: "memory");
#pragma unroll
  for (int i = 0; i < 5; ++i) {
    const int pc = wave * 5 + i;
    const int rb = pc >> 2, cb = pc & 3;
    const int rbs = rb > 8 ? 8 : rb;
    dma16(rfl(lds0 + OFF_Q + (rb >> 1) * KSLOT + (rb & 1) * 4160 + cb * 1024), vsrc_row, srdQ, rfl(rbs * 8 * ROWB + cb * 128));
  }

  if (wave < 4) {
    const unsigned kbase = lds0 + OFF_K + w * 2 * KSLOT;
    auto dma_k = [&](int t, int pc) {
      const int rb = pc & 1, cb = pc >> 1;
      dma16(kbase + (t & 1) * KSLOT + rb * 4160 + cb * 1024, vsrc_row, srdK, (t * KT + w * 16 + rb * 8) * ROWB + cb * 128);
    };
#pragma unroll
    for (int pc = 0; pc < 8; ++pc) dma_k(0, pc);
#pragma unroll
    for (int pc = 0; pc < 8; ++pc) dma_k(1, pc);
    wait_vm<16>();
    wg_barrier();
    STAMP();
    const int frag_off = (r16 >> 3) * 4160 + (r16 & 7) * 128 + ((g ^ ((r16 >> 1) & 3)) << 4);
    bf16x8 qf[NRT][8];
#pragma unroll
    for (int rt = 0; rt < NRT; ++rt)
#pragma unroll
      for (int s = 0; s < 8; ++s)
        qf[rt][s] = *(LDS_AS bf16x8*)(lds + OFF_Q + rt * KSLOT + frag_off + (s >> 1) * 1024 + (s & 1) * 64);
    LDS_AS char* kp = lds + OFF_K + w * 2 * KSLOT + frag_off;
    LDS_AS char* pp = lds + OFF_P + (w >> 1) * 1024 + lane * 16 + (w & 1) * 8;
    LDS_AS float* mxp = (LDS_AS float*)(lds + OFF_MX) + r16;
    float mthr[NRT], mcs[NRT], lsum[NRT];
    f32x4 sacc[2][NRT];
    bf16x8 kf0, kf1;
#pragma unroll
    for (int rt = 0; rt < NRT; ++rt) { mthr[rt] = -INFINITY; mcs[rt] = INFINITY; lsum[rt] = 0.f; }
    if (PRIO_QK) __builtin_amdgcn_s_setprio(PRIO_QK);

    auto check = [&](int t, int par, int gs) {
      if (rfl(gs) == t) {
#pragma unroll
        for (int rt = 0; rt < NRT; ++rt) {
          const float mx = mxp[par * 80 + rt * 16];
          const bool gr = mx > mthr[rt];
          const float mcn = gr ? -mx * SC_LOG2E : mcs[rt];
          lsum[rt] *= __builtin_amdgcn_exp2f(mcn - mcs[rt]);
          mcs[rt] = mcn;
          mthr[rt] = gr ? mx + RESCALE_THR : mthr[rt];
        }
      }
    };
    auto sm_rt = [&](int par, int rt) {
      const float mc = mcs[rt];
      float p[4];
#pragma unroll
      for (int i = 0; i < 4; ++i) p[i] = __builtin_amdgcn_exp2f(__builtin_fmaf(sacc[par][rt][i], SC_LOG2E, mc));
      float a = p[0] + p[1], b = p[2] + p[3];
      asm volatile("" : "+v"(a));
      asm volatile("" : "+v"(b));
      a += b;
      asm volatile("" : "+v"(a));
      lsum[rt] += a;
      asm volatile("" : "+v"(lsum[rt]));
      u32x2 pk = {pk2(p[0], p[1]), pk2(p[2], p[3])};
      *(LDS_AS u32x2*)(pp + par * PBUF + rt * 2048) = pk;
    };
    auto kread = [&](int par, int s) { return *(LDS_AS bf16x8*)(kp + par * KSLOT + (s >> 1) * 1024 + (s & 1) * 64); };
    auto iter = [&](int t, int par, bool sm, bool more, int nextwait) {
      bf16x8 kf[8];
      kf[0] = kf0; kf[1] = kf1;
      int gs = 0;
      if (sm) gs = gsp[par ^ 1];
#pragma unroll
      for (int s = 0; s < 6; ++s) {
        kf[s + 2] = kread(par, s + 2);
#pragma unroll
        for (int rt = 0; rt < NRT; ++rt) sacc[par][rt] = mfma16(kf[s], qf[rt][s], s == 0 ? f32x4{0.f, 0.f, 0.f, 0.f} : sacc[par][rt]);
        if (sm && s == 0) { SB0(); check(t - 1, par ^ 1, gs); }
        if (sm && s >= 1) {
          sm_rt(par ^ 1, s - 1);
#pragma unroll
          for (int i = 0; i < 5; ++i) {
            __builtin_amdgcn_sched_group_barrier(0x008, 1, 0);
            __builtin_amdgcn_sched_group_barrier(0x002, NVA, 0);
          }
        }
        SB0();
        if (more && s >= 1) dma_k(t + 2, s - 1);
        SB0();
      }
      float m[NRT];
      bool ex = false;
#pragma unroll
      for (int rt = 0; rt <= NRT; ++rt) {
        if (rt < NRT) {
          sacc[par][rt] = mfma16(kf[6], qf[rt][6], sacc[par][rt]);
          sacc[par][rt] = mfma16(kf[7], qf[rt][7], sacc[par][rt]);
        }
        SB0();
        if (rt == 2) {
          if (nextwait == 8) wait_vm<5>(); else if (nextwait == 0) wait_vm<0>();
          if (nextwait >= 0) { kf0 = kread(par ^ 1, 0); kf1 = kread(par ^ 1, 1); }
        }
        if (rt > 0) {
          const f32x4 x = sacc[par][rt - 1];
          m[rt - 1] = fmax2(fmax2(fmax2(x[0], x[1]), x[2]), x[3]);
          ex |= m[rt - 1] > mthr[rt - 1];
        }
        SB0();
      }
      if (__builtin_amdgcn_ballot_w64(ex)) {
#pragma unroll
        for (int rt = 0; rt < NRT; ++rt)
          __hip_atomic_fetch_max(mxp + par * 80 + rt * 16, m[rt], __ATOMIC_RELAXED, __HIP_MEMORY_SCOPE_WORKGROUP);
        gsp[par] = t;
      }
    };
    STAMP();
    wait_vm<8>();
    kf0 = kread(0, 0); kf1 = kread(0, 1);
    STAMP();
    iter(0, 0, false, true, 8);
    STAMP();
    wg_barrier();
#pragma unroll 1
    for (int t = 1; t < NT - 3; t += 2) {
      STAMP();
      iter(t, 1, true, true, 8);
      STAMP();
      wg_barrier();
      STAMP();
      iter(t + 1, 0, true, true, 8);
      STAMP();
      wg_barrier();
    }
    iter(13, 1, true, true, 8);   wg_barrier();
    iter(14, 0, true, false, 0);  wg_barrier();
    iter(15, 1, true, false, -1); wg_barrier();
    check(NT - 1, 1, gsp[1]);
#pragma unroll
    for (int rt = 0; rt < NRT; ++rt) sm_rt(1, rt);
#pragma unroll
    for (int rt = 0; rt < NRT; ++rt) *(LDS_AS float*)(lds + OFF_L + ((rt * 16 + r16) * 16 + w * 4 + g) * 4) = lsum[rt];
    wg_barrier();
    STAMP();
    STAMP_DUMP();
  } else {
    const unsigned vsrc_v = (16 * (lane >> 4) + ((lane >> 3) & 1)) * ROWB + (lane & 7) * 16 + w * 128;
    const unsigned vbase = lds0 + OFF_V0 + w * VSLOT;
    auto dma_v = [&](int t, int pc) {
      dma16(vbase + (t & 1) * (4 * VSLOT) + pc * 1056, vsrc_v, srdV, t * KT * ROWB + pc * 1024);
    };
    auto dma_k2 = [&](int t, int pc) {
      const int rb = pc & 1, cb = pc >> 1;
      if (t < NT) dma16(lds0 + OFF_K + w * 2 * KSLOT + (t & 1) * KSLOT + rb * 4160 + cb * 1024, vsrc_row, srdK, (t * KT + w * 16 + rb * 8) * ROWB + cb * 128);
    };
    const unsigned vsrc_pf = (jq * 64 + lane) * 128 + (w & 1) * 64;
    const __amdgpu_buffer_rsrc_t srdPF = __builtin_amdgcn_make_buffer_rsrc((void*)((w < 2 ? K : V) + (size_t)bh * SK * DH), (short)0, SK * ROWB, 0x00020000);
    const int pf_add = w < 2 ? 2 : 0;
    auto prefetch = [&](int n) {
    };
#pragma unroll
    for (int pc = 0; pc < 8; ++pc) dma_v(0, pc);
    wait_vm<8>();
    wg_barrier();
    STAMP();
    const int kk = 4 * g + (r16 >> 2), pq = lane & 3;
    LDS_AS char* vp = lds + OFF_V0 + w * VSLOT + (kk >> 1) * 1056 + (kk & 1) * 128 + pq * 8;
    LDS_AS char* pp = lds + OFF_P + lane * 16;
    LDS_AS float* mxp = (LDS_AS float*)(lds + OFF_MX) + r16;
    if (PRIO_PV) __builtin_amdgcn_s_setprio(PRIO_PV);
    f32x4 oacc[4][NRT];
    float mthr[NRT], mcs[NRT];
#pragma unroll
    for (int rt = 0; rt < NRT; ++rt) {
      mthr[rt] = -INFINITY; mcs[rt] = INFINITY;
#pragma unroll
      for (int c = 0; c < 4; ++c) oacc[c][rt] = f32x4{0.f, 0.f, 0.f, 0.f};
    }
    bf16x8 vf[2][4];
    auto load_v = [&](int par) {
#pragma unroll
      for (int ks = 0; ks < 2; ++ks)
#pragma unroll
        for (int c = 0; c < 4; ++c) {
          const s16x4 a = __builtin_amdgcn_ds_read_tr16_b64_v4i16((LDS_AS s16x4*)(vp + par * (4 * VSLOT) + (2 * ks) * 256 + c * 32));
          const s16x4 b = __builtin_amdgcn_ds_read_tr16_b64_v4i16((LDS_AS s16x4*)(vp + par * (4 * VSLOT) + (2 * ks + 1) * 256 + c * 32));
          vf[ks][c] = __builtin_bit_cast(bf16x8, __builtin_shufflevector(a, b, 0, 1, 2, 3, 4, 5, 6, 7));
        }
    };
    int gs = 0;
    auto rescale = [&](int par, int t) {
      if (rfl(gs) == t) {
        float mx[NRT];
#pragma unroll
        for (int rt = 0; rt < NRT; ++rt) mx[rt] = mxp[par * 80 + rt * 16];
#pragma unroll
        for (int rt = 0; rt < NRT; ++rt) {
          const bool gr = mx[rt] > mthr[rt];
          const float mcn = gr ? -mx[rt] * SC_LOG2E : mcs[rt];
          const float al = __builtin_amdgcn_exp2f(mcn - mcs[rt]);
          mcs[rt] = mcn;
          mthr[rt] = gr ? mx[rt] + RESCALE_THR : mthr[rt];
#pragma unroll
          for (int c = 0; c < 4; ++c) oacc[c][rt] *= al;
        }
      }
    };
    auto pv = [&](int tn, int par, bool more) {
      bf16x8 pf[2][NRT];
      gs = gsp[par ^ 1];
#pragma unroll
      for (int ks = 0; ks < 2; ++ks)
#pragma unroll
        for (int rt = 0; rt < NRT; ++rt) pf[ks][rt] = *(LDS_AS bf16x8*)(pp + par * PBUF + rt * 2048 + ks * 1024);
      SB0();
      if (more) dma_k2(tn + 1, 5);
      SB0();
#pragma unroll
      for (int ks = 0; ks < 2; ++ks)
#pragma unroll
        for (int rt = 0; rt < NRT; ++rt) {
#pragma unroll
          for (int c = 0; c < 4; ++c) oacc[c][rt] = mfma16(vf[ks][c], pf[ks][rt], oacc[c][rt]);
          SB0();
          const int i = ks * NRT + rt;
          if (more) { if (i < 2) dma_k2(tn + 1, 6 + i); else dma_v(tn, i - 2); }
          SB0();
        }
    };
    constexpr int PFN = 0, PF0 = 0;
    wg_barrier();
#pragma unroll
    for (int pc = 5; pc < 8; ++pc) dma_k2(2, pc);
#pragma unroll
    for (int pc = 0; pc < 8; ++pc) dma_v(1, pc);
    gs = gsp[0];
    rescale(0, 0);
    wait_vm<8 + PF0>();
    load_v(0);
    prefetch(1 + PF_DIST);
    wg_barrier();
#pragma unroll 1
    for (int t = 2; t < NT; t += 2) {
      STAMP();
      pv(t, 0, true);
      rescale(1, t - 1);
      wait_vm<8 + PFN>();
      load_v(1);
      prefetch(t + PF_DIST);
      STAMP();
      wg_barrier();
      STAMP();
      pv(t + 1, 1, true);
      rescale(0, t);
      wait_vm<8 + PFN>();
      load_v(0);
      prefetch(t + 1 + PF_DIST);
      STAMP();
      wg_barrier();
    }
    pv(0, 0, false);
    rescale(1, NT - 1);
    wait_vm<0>();
    load_v(1);
    wg_barrier();
    pv(0, 1, false);
    STAMP();
#pragma unroll
    for (int rt = 0; rt < NRT; ++rt) {
      const LDS_AS f32x4* lp = (const LDS_AS f32x4*)(lds + OFF_L + (rt * 16 + r16) * 64);
      const f32x4 s4 = (lp[0] + lp[1]) + (lp[2] + lp[3]);
      const float inv = __builtin_amdgcn_rcpf((s4[0] + s4[1]) + (s4[2] + s4[3]));
      const int row = rt * 16 + r16;
#pragma unroll
      for (int u = 0; u < 2; ++u) {
        const f32x4 a = oacc[2 * u][rt] * inv, b = oacc[2 * u + 1][rt] * inv;
        const auto x = __builtin_amdgcn_permlane16_swap(pk2(a[0], a[1]), pk2(b[0], b[1]), false, false);
        const auto y = __builtin_amdgcn_permlane16_swap(pk2(a[2], a[3]), pk2(b[2], b[3]), false, false);
        const u32x4 o = {x[0], y[0], x[1], y[1]};
        if (row < ROWS_WG)
          __builtin_nontemporal_store(o, (u32x4*)(O + (size_t)(row0 + row) * DH + w * 64 + u * 32 + (g & 1) * 16 + (g >> 1) * 8));
      }
    }
    STAMP();
    STAMP_DUMP();
  }
}

extern "C" void kernel_launch(void* const* d_in, const int* in_sizes, int n_in, void* d_out, int out_size,
                              void* d_ws, size_t ws_size, hipStream_t stream) {
  (void)in_sizes; (void)n_in; (void)out_size; (void)d_ws; (void)ws_size;
  gqa_fwd<<<NB * HKV * 4, 512, 0, stream>>>((const __bf16*)d_in[0], (const __bf16*)d_in[1], (const __bf16*)d_in[2], (__bf16*)d_out);
}
```

```cpp
#include <hip/hip_runtime.h>
#include <hip/hip_bf16.h>

typedef __attribute__((ext_vector_type(8))) __bf16 bf16x8;
typedef __attribute__((ext_vector_type(2))) __bf16 bf16x2;
typedef __attribute__((ext_vector_type(4))) float f32x4;
typedef __attribute__((ext_vector_type(2))) float f32x2;
typedef __attribute__((ext_vector_type(4))) short s16x4;
typedef __attribute__((ext_vector_type(8))) short s16x8;
typedef __attribute__((ext_vector_type(4))) unsigned u32x4;
typedef __attribute__((ext_vector_type(2))) unsigned u32x2;
#define LDS_AS __attribute__((address_space(3)))
#define DI __device__ __forceinline__

constexpr int NB = 8, HQ = 16, HKV = 8, SQ = 144, SK = 1024, DH = 256;
constexpr int ROWS_KV = (HQ / HKV) * SQ;
constexpr int ROWS_WG = ROWS_KV / 4;
constexpr int NRT = 5;
constexpr int KT = 64;
constexpr int NT = SK / KT;
constexpr int ROWB = DH * 2;

constexpr int KSLOT = 2 * 4096 + 64;
constexpr int VSLOT = 8 * 1056;
constexpr int PBUF = NRT * 2 * 1024;
constexpr int OFF_K = 0;
constexpr int OFF_V0 = OFF_K + 8 * KSLOT;
constexpr int OFF_V1 = OFF_V0 + 4 * VSLOT;
constexpr int OFF_P = OFF_V1 + 4 * VSLOT;
constexpr int OFF_MX = OFF_P + 2 * PBUF;
constexpr int OFF_L = OFF_MX + 2 * 80 * 4;
constexpr int OFF_GS = OFF_L + 80 * 16 * 4;
constexpr int LDS_BYTES = OFF_GS + 16;
constexpr int OFF_Q = OFF_V1;
static_assert(OFF_Q + NRT * KSLOT <= OFF_MX, "Q staging too large");
static_assert(LDS_BYTES <= 163840, "LDS");

constexpr float SC_LOG2E = 0.0625f * 1.4426950408889634f;
constexpr float RESCALE_THR = 8.0f / SC_LOG2E;

#define POL "nt"
#define NVA 3

DI unsigned rfl(unsigned x) { return __builtin_amdgcn_readfirstlane(x); }
DI void dma16(unsigned m0v, unsigned voff, __amdgpu_buffer_rsrc_t srd, unsigned soff) {
  asm volatile("s_mov_b32 m0, %0\n\ts_nop 0\n\tbuffer_load_dwordx4 %1, %2, %3 offen " POL " lds"
               :: "s"(m0v), "v"(voff), "s"(srd), "s"(soff) : "memory");
}
template <int N> DI void wait_vm() { asm volatile("s_waitcnt vmcnt(%0)" :: "i"(N) : "memory"); }
DI void wg_barrier() {
  asm volatile("s_waitcnt lgkmcnt(0)\n\ts_barrier" ::: "memory");
}
#define SB0() __builtin_amdgcn_sched_barrier(0)
DI float fmax2(float a, float b) { return __builtin_elementwise_maximum(a, b); }
DI unsigned pk2(float a, float b) {
  f32x2 v = {a, b};
  return __builtin_bit_cast(unsigned, __builtin_convertvector(v, bf16x2));
}
DI f32x4 mfma16(bf16x8 a, bf16x8 b, f32x4 c) { return __builtin_amdgcn_mfma_f32_16x16x32_bf16(a, b, c, 0, 0, 0); }

__global__ __launch_bounds__(512, 2) void gqa_fwd(const __bf16* __restrict__ Q, const __bf16* __restrict__ K,
                                                  const __bf16* __restrict__ V, __bf16* __restrict__ O) {
  __shared__ __attribute__((aligned(16))) char smem[LDS_BYTES];
  LDS_AS char* lds = (LDS_AS char*)smem;
  const unsigned lds0 = (unsigned)(size_t)lds;
  const int tid = threadIdx.x;
  const int lane = tid & 63;
  const int wave = rfl(tid >> 6);
  const int w = wave & 3;
  const int r16 = lane & 15, g = lane >> 4;
  const int bid = blockIdx.x;
  const int bh = (bid & 7) * 8 + (bid >> 5);
  const int jq = (bid >> 3) & 3;
  const int row0 = bh * ROWS_KV + jq * ROWS_WG;

  const __amdgpu_buffer_rsrc_t srdQ = __builtin_amdgcn_make_buffer_rsrc((void*)(Q + (size_t)row0 * DH), (short)0, ROWS_WG * ROWB, 0x00020000);
  const __amdgpu_buffer_rsrc_t srdK = __builtin_amdgcn_make_buffer_rsrc((void*)(K + (size_t)bh * SK * DH), (short)0, SK * ROWB, 0x00020000);
  const __amdgpu_buffer_rsrc_t srdV = __builtin_amdgcn_make_buffer_rsrc((void*)(V + (size_t)bh * SK * DH), (short)0, SK * ROWB, 0x00020000);

  const unsigned vsrc_row = (lane >> 3) * ROWB + (((lane & 7) ^ ((lane >> 4) & 3)) << 4);
  if (tid < 160) *(LDS_AS float*)(lds + OFF_MX + tid * 4) = -INFINITY;
  LDS_AS int* const gsp = (LDS_AS int*)(lds + OFF_GS);
  if (tid < 2) gsp[tid] = -1;

  asm volatile("s_nop 4" ::: "memory");
#pragma unroll
  for (int i = 0; i < 5; ++i) {
    const int pc = wave * 5 + i;
    const int rb = pc >> 2, cb = pc & 3;
    const int rbs = rb > 8 ? 8 : rb;
    dma16(rfl(lds0 + OFF_Q + (rb >> 1) * KSLOT + (rb & 1) * 4160 + cb * 1024), vsrc_row, srdQ, rfl(rbs * 8 * ROWB + cb * 128));
  }

  if (wave < 4) {
    const unsigned kbase = lds0 + OFF_K + w * 2 * KSLOT;
    auto dma_k = [&](int t, int pc) {
      const int rb = pc & 1, cb = pc >> 1;
      dma16(kbase + (t & 1) * KSLOT + rb * 4160 + cb * 1024, vsrc_row, srdK, (t * KT + w * 16 + rb * 8) * ROWB + cb * 128);
    };
#pragma unroll
    for (int pc = 0; pc < 8; ++pc) dma_k(0, pc);
    wait_vm<8>();
    wg_barrier();
#pragma unroll
    for (int pc = 0; pc < 8; ++pc) dma_k(1, pc);
    const int frag_off = (r16 >> 3) * 4160 + (r16 & 7) * 128 + ((g ^ ((r16 >> 1) & 3)) << 4);
    bf16x8 qf[NRT][8];
#pragma unroll
    for (int rt = 0; rt < NRT; ++rt)
#pragma unroll
      for (int s = 0; s < 8; ++s)
        qf[rt][s] = *(LDS_AS bf16x8*)(lds + OFF_Q + rt * KSLOT + frag_off + (s >> 1) * 1024 + (s & 1) * 64);
    LDS_AS char* kp = lds + OFF_K + w * 2 * KSLOT + frag_off;
    LDS_AS char* pp = lds + OFF_P + (w >> 1) * 1024 + lane * 16 + (w & 1) * 8;
    LDS_AS float* mxp = (LDS_AS float*)(lds + OFF_MX) + r16;
    float mthr[NRT], mcs[NRT], lsum[NRT];
    f32x4 sacc[2][NRT];
    bf16x8 kf0, kf1;
#pragma unroll
    for (int rt = 0; rt < NRT; ++rt) { mthr[rt] = -INFINITY; mcs[rt] = INFINITY; lsum[rt] = 0.f; }

    auto check = [&](int t, int par, int gs) {
      if (rfl(gs) == t) {
#pragma unroll
        for (int rt = 0; rt < NRT; ++rt) {
          const float mx = mxp[par * 80 + rt * 16];
          const bool gr = mx > mthr[rt];
          const float mcn = gr ? -mx * SC_LOG2E : mcs[rt];
          lsum[rt] *= __builtin_amdgcn_exp2f(mcn - mcs[rt]);
          mcs[rt] = mcn;
          mthr[rt] = gr ? mx + RESCALE_THR : mthr[rt];
        }
      }
    };
    auto sm_rt = [&](int par, int rt) {
      const float mc = mcs[rt];
      float p[4];
#pragma unroll
      for (int i = 0; i < 4; ++i) p[i] = __builtin_amdgcn_exp2f(__builtin_fmaf(sacc[par][rt][i], SC_LOG2E, mc));
      float a = p[0] + p[1], b = p[2] + p[3];
      asm volatile("" : "+v"(a));
      asm volatile("" : "+v"(b));
      a += b;
      asm volatile("" : "+v"(a));
      lsum[rt] += a;
      asm volatile("" : "+v"(lsum[rt]));
      u32x2 pk = {pk2(p[0], p[1]), pk2(p[2], p[3])};
      *(LDS_AS u32x2*)(pp + par * PBUF + rt * 2048) = pk;
    };
    auto kread = [&](int par, int s) { return *(LDS_AS bf16x8*)(kp + par * KSLOT + (s >> 1) * 1024 + (s & 1) * 64); };
    auto iter = [&](int t, int par, bool sm, bool more, int nextwait) {
      bf16x8 kf[8];
      kf[0] = kf0; kf[1] = kf1;
      int gs = 0;
      if (sm) gs = gsp[par ^ 1];
#pragma unroll
      for (int s = 0; s < 6; ++s) {
        kf[s + 2] = kread(par, s + 2);
#pragma unroll
        for (int rt = 0; rt < NRT; ++rt) sacc[par][rt] = mfma16(kf[s], qf[rt][s], s == 0 ? f32x4{0.f, 0.f, 0.f, 0.f} : sacc[par][rt]);
        if (sm && s == 0) { SB0(); check(t - 1, par ^ 1, gs); }
        if (sm && s >= 1) {
          sm_rt(par ^ 1, s - 1);
#pragma unroll
          for (int i = 0; i < 5; ++i) {
            __builtin_amdgcn_sched_group_barrier(0x008, 1, 0);
            __builtin_amdgcn_sched_group_barrier(0x002, NVA, 0);
          }
        }
        SB0();
        if (more && s >= 1) dma_k(t + 2, s - 1);
        SB0();
      }
      float m[NRT];
      bool ex = false;
#pragma unroll
      for (int rt = 0; rt <= NRT; ++rt) {
        if (rt < NRT) {
          sacc[par][rt] = mfma16(kf[6], qf[rt][6], sacc[par][rt]);
          sacc[par][rt] = mfma16(kf[7], qf[rt][7], sacc[par][rt]);
        }
        SB0();
        if (rt == 2) {
          if (nextwait == 8) wait_vm<5>(); else if (nextwait == 0) wait_vm<0>();
          if (nextwait >= 0) { kf0 = kread(par ^ 1, 0); kf1 = kread(par ^ 1, 1); }
        }
        if (rt > 0) {
          const f32x4 x = sacc[par][rt - 1];
          m[rt - 1] = fmax2(fmax2(fmax2(x[0], x[1]), x[2]), x[3]);
          ex |= m[rt - 1] > mthr[rt - 1];
        }
        SB0();
      }
      if (__builtin_amdgcn_ballot_w64(ex)) {
#pragma unroll
        for (int rt = 0; rt < NRT; ++rt)
          __hip_atomic_fetch_max(mxp + par * 80 + rt * 16, m[rt], __ATOMIC_RELAXED, __HIP_MEMORY_SCOPE_WORKGROUP);
        gsp[par] = t;
      }
    };
    wait_vm<8>();
    kf0 = kread(0, 0); kf1 = kread(0, 1);
    iter(0, 0, false, true, 8);
    wg_barrier();
#pragma unroll 1
    for (int t = 1; t < NT - 3; t += 2) {
      iter(t, 1, true, true, 8);
      wg_barrier();
      iter(t + 1, 0, true, true, 8);
      wg_barrier();
    }
    iter(13, 1, true, true, 8);   wg_barrier();
    iter(14, 0, true, false, 0);  wg_barrier();
    iter(15, 1, true, false, -1); wg_barrier();
    check(NT - 1, 1, gsp[1]);
#pragma unroll
    for (int rt = 0; rt < NRT; ++rt) sm_rt(1, rt);
#pragma unroll
    for (int rt = 0; rt < NRT; ++rt) *(LDS_AS float*)(lds + OFF_L + ((rt * 16 + r16) * 16 + w * 4 + g) * 4) = lsum[rt];
    wg_barrier();
  } else {
    const unsigned vsrc_v = (16 * (lane >> 4) + ((lane >> 3) & 1)) * ROWB + (lane & 7) * 16 + w * 128;
    const unsigned vbase = lds0 + OFF_V0 + w * VSLOT;
    auto dma_v = [&](int t, int pc) {
      dma16(vbase + (t & 1) * (4 * VSLOT) + pc * 1056, vsrc_v, srdV, t * KT * ROWB + pc * 1024);
    };
    auto dma_k2 = [&](int t, int pc) {
      const int rb = pc & 1, cb = pc >> 1;
      if (t < NT) dma16(lds0 + OFF_K + w * 2 * KSLOT + (t & 1) * KSLOT + rb * 4160 + cb * 1024, vsrc_row, srdK, (t * KT + w * 16 + rb * 8) * ROWB + cb * 128);
    };
    wait_vm<0>();
    wg_barrier();
#pragma unroll
    for (int pc = 0; pc < 8; ++pc) dma_v(0, pc);
    const int kk = 4 * g + (r16 >> 2), pq = lane & 3;
    LDS_AS char* vp = lds + OFF_V0 + w * VSLOT + (kk >> 1) * 1056 + (kk & 1) * 128 + pq * 8;
    LDS_AS char* pp = lds + OFF_P + lane * 16;
    LDS_AS float* mxp = (LDS_AS float*)(lds + OFF_MX) + r16;
    f32x4 oacc[4][NRT];
    float mthr[NRT], mcs[NRT];
#pragma unroll
    for (int rt = 0; rt < NRT; ++rt) {
      mthr[rt] = -INFINITY; mcs[rt] = INFINITY;
#pragma unroll
      for (int c = 0; c < 4; ++c) oacc[c][rt] = f32x4{0.f, 0.f, 0.f, 0.f};
    }
    bf16x8 vf[2][4];
    auto load_v = [&](int par) {
#pragma unroll
      for (int ks = 0; ks < 2; ++ks)
#pragma unroll
        for (int c = 0; c < 4; ++c) {
          const s16x4 a = __builtin_amdgcn_ds_read_tr16_b64_v4i16((LDS_AS s16x4*)(vp + par * (4 * VSLOT) + (2 * ks) * 256 + c * 32));
          const s16x4 b = __builtin_amdgcn_ds_read_tr16_b64_v4i16((LDS_AS s16x4*)(vp + par * (4 * VSLOT) + (2 * ks + 1) * 256 + c * 32));
          vf[ks][c] = __builtin_bit_cast(bf16x8, __builtin_shufflevector(a, b, 0, 1, 2, 3, 4, 5, 6, 7));
        }
    };
    int gs = 0;
    auto rescale = [&](int par, int t) {
      if (rfl(gs) == t) {
        float mx[NRT];
#pragma unroll
        for (int rt = 0; rt < NRT; ++rt) mx[rt] = mxp[par * 80 + rt * 16];
#pragma unroll
        for (int rt = 0; rt < NRT; ++rt) {
          const bool gr = mx[rt] > mthr[rt];
          const float mcn = gr ? -mx[rt] * SC_LOG2E : mcs[rt];
          const float al = __builtin_amdgcn_exp2f(mcn - mcs[rt]);
          mcs[rt] = mcn;
          mthr[rt] = gr ? mx[rt] + RESCALE_THR : mthr[rt];
#pragma unroll
          for (int c = 0; c < 4; ++c) oacc[c][rt] *= al;
        }
      }
    };
    auto pv = [&](int tn, int par, bool more) {
      bf16x8 pf[2][NRT];
      gs = gsp[par ^ 1];
#pragma unroll
      for (int ks = 0; ks < 2; ++ks)
#pragma unroll
        for (int rt = 0; rt < NRT; ++rt) pf[ks][rt] = *(LDS_AS bf16x8*)(pp + par * PBUF + rt * 2048 + ks * 1024);
      SB0();
      if (more) dma_k2(tn + 1, 5);
      SB0();
#pragma unroll
      for (int ks = 0; ks < 2; ++ks)
#pragma unroll
        for (int rt = 0; rt < NRT; ++rt) {
#pragma unroll
          for (int c = 0; c < 4; ++c) oacc[c][rt] = mfma16(vf[ks][c], pf[ks][rt], oacc[c][rt]);
          SB0();
          const int i = ks * NRT + rt;
          if (more) { if (i < 2) dma_k2(tn + 1, 6 + i); else dma_v(tn, i - 2); }
          SB0();
        }
    };
    wg_barrier();
#pragma unroll
    for (int pc = 5; pc < 8; ++pc) dma_k2(2, pc);
#pragma unroll
    for (int pc = 0; pc < 8; ++pc) dma_v(1, pc);
    gs = gsp[0];
    rescale(0, 0);
    wait_vm<8>();
    load_v(0);
    wg_barrier();
#pragma unroll 1
    for (int t = 2; t < NT; t += 2) {
      pv(t, 0, true);
      rescale(1, t - 1);
      wait_vm<8>();
      load_v(1);
      wg_barrier();
      pv(t + 1, 1, true);
      rescale(0, t);
      wait_vm<8>();
      load_v(0);
      wg_barrier();
    }
    pv(0, 0, false);
    rescale(1, NT - 1);
    wait_vm<0>();
    load_v(1);
    wg_barrier();
    pv(0, 1, false);
#pragma unroll
    for (int rt = 0; rt < NRT; ++rt) {
      const LDS_AS f32x4* lp = (const LDS_AS f32x4*)(lds + OFF_L + (rt * 16 + r16) * 64);
      const f32x4 s4 = (lp[0] + lp[1]) + (lp[2] + lp[3]);
      const float inv = __builtin_amdgcn_rcpf((s4[0] + s4[1]) + (s4[2] + s4[3]));
      const int row = rt * 16 + r16;
#pragma unroll
      for (int u = 0; u < 2; ++u) {
        const f32x4 a = oacc[2 * u][rt] * inv, b = oacc[2 * u + 1][rt] * inv;
        const auto x = __builtin_amdgcn_permlane16_swap(pk2(a[0], a[1]), pk2(b[0], b[1]), false, false);
        const auto y = __builtin_amdgcn_permlane16_swap(pk2(a[2], a[3]), pk2(b[2], b[3]), false, false);
        const u32x4 o = {x[0], y[0], x[1], y[1]};
        if (row < ROWS_WG)
          __builtin_nontemporal_store(o, (u32x4*)(O + (size_t)(row0 + row) * DH + w * 64 + u * 32 + (g & 1) * 16 + (g >> 1) * 8));
      }
    }
  }
}

extern "C" void kernel_launch(void* const* d_in, const int* in_sizes, int n_in, void* d_out, int out_size,
                              void* d_ws, size_t ws_size, hipStream_t stream) {
  (void)in_sizes; (void)n_in; (void)out_size; (void)d_ws; (void)ws_size;
  gqa_fwd<<<NB * HKV * 4, 512, 0, stream>>>((const __bf16*)d_in[0], (const __bf16*)d_in[1], (const __bf16*)d_in[2], (__bf16*)d_out);
}
```
